```python
import torch

def _softmax(x, axis=-1):
    return torch.softmax(x, dim=axis)
def _gelu(x):
    return torch.nn.functional.gelu(x, approximate="tanh")
def _silu(x):
    return torch.nn.functional.silu(x)
def _sigmoid(x):
    return torch.sigmoid(x)
def _layernorm(x, axis=-1, eps=1e-5):
    m = x.mean(dim=axis, keepdim=True)
    v = x.var(dim=axis, keepdim=True, unbiased=False)
    return (x - m) / torch.sqrt(v + eps)
def _scatter_add(tgt, idx, src, axis=0):
    return tgt.clone().index_add_(axis, idx, src)
def _segment_sum(src, seg, n):
    out = torch.zeros((n,) + src.shape[1:], dtype=src.dtype)
    return out.index_add_(0, seg, src)
def _pad(x, pw):
    flat = tuple(v for pair in reversed(pw) for v in pair)
    return torch.nn.functional.pad(x, flat)

kernel_name = 'gated_mix__mha_block_cached__dense_activation_relu__decode'

def setup_inputs():

    _w = (lambda a: (lambda t: t.to(torch.bfloat16) if t.is_floating_point() else t)(torch.as_tensor(a)))
    def _A_setup():
        B, Sq, D = 2, 1, 448
        H, Sk, Dh = 16, 4096, 256
        return {
            "x": _w((lambda *s: torch.randn(*s, dtype=torch.bfloat16) * 0.05)(B, Sq, D)),
            "k_cache": _w((lambda *s: torch.randn(*s, dtype=torch.bfloat16) * 6.4)(B, H, Sk, Dh)),
            "v_cache": _w((lambda *s: torch.randn(*s, dtype=torch.bfloat16) * 0.4)(B, H, Sk, Dh)),
            "wq": _w((lambda *s: torch.randn(*s, dtype=torch.bfloat16) * 6.4)(D, H * Dh) * (D ** -0.5)),
            "wo": _w((lambda *s: torch.randn(*s, dtype=torch.bfloat16) * 0.4)(H * Dh, D) * ((H * Dh) ** -0.5)),
        }
    _A_in = _A_setup()

    def _B_setup():
        B, S, D, Dff = 2, 1, 448, 1792
        return {
            "x": _w((lambda *s: torch.randn(*s, dtype=torch.bfloat16) * 0.05)(B, S, D)),
            "w1": _w((lambda *s: torch.randn(*s, dtype=torch.bfloat16) * 0.05)(D, Dff)),
            "w2": _w((lambda *s: torch.randn(*s, dtype=torch.bfloat16) * 0.05)(Dff, D)),
        }
    _B_in = _B_setup()

    return {
        "A_x": _A_in["x"],
        "A_k_cache": _A_in["k_cache"],
        "A_v_cache": _A_in["v_cache"],
        "A_wq": _A_in["wq"],
        "A_wo": _A_in["wo"],
        "B_x": _B_in["x"],
        "B_w1": _B_in["w1"],
        "B_w2": _B_in["w2"],
        "gate_w": _w((lambda *s: torch.randn(*s, dtype=torch.bfloat16) * 0.05)(448) * 16.0),
    }

def reference(A_x, A_k_cache, A_v_cache, A_wq, A_wo, B_x, B_w1, B_w2, gate_w):
    def _A_ref(x, k_cache, v_cache, wq, wo):
        B, Sq, D = x.shape
        H, Dh = 16, 256
        q = (lambda x, *d: x.permute(*d))((x @ wq).reshape(B, Sq, H, Dh), 0, 2, 1, 3)
        scores = torch.einsum("bhqd,bhkd->bhqk", q, k_cache) * (Dh ** -0.5)
        p = _softmax(scores, axis=-1)
        o = torch.einsum("bhqk,bhkd->bhqd", p, v_cache)
        return (lambda x, *d: x.permute(*d))(o, 0, 2, 1, 3).reshape(B, Sq, H * Dh) @ wo

    def _B_ref(x, w1, w2):
        h = torch.einsum("bsd,df->bsf", x, w1)
        zero = torch.zeros_like(h)
        h = torch.maximum(h, zero)
        return torch.einsum("bsf,fd->bsd", h, w2)

    x = A_x
    g = _sigmoid((x * gate_w).sum(-1, keepdims=True))
    a = _A_ref(x, A_k_cache, A_v_cache, A_wq, A_wo)
    b = _B_ref(x, B_w1, B_w2)
    return g * a + (1 - g) * b
```

```cpp
#include <hip/hip_runtime.h>
#include <hip/hip_bf16.h>

typedef unsigned u32;
typedef unsigned long long u64;
typedef u32 u32x2 __attribute__((ext_vector_type(2)));
typedef u32 u32x4 __attribute__((ext_vector_type(4)));
typedef float f32x2 __attribute__((ext_vector_type(2)));
typedef float f32x4 __attribute__((ext_vector_type(4)));
typedef __bf16 bf16x2 __attribute__((ext_vector_type(2)));
typedef __bf16 bf16x8 __attribute__((ext_vector_type(8)));
typedef __amdgpu_buffer_rsrc_t rsrc_t;
typedef __attribute__((address_space(1))) u64 gu64;
typedef __attribute__((address_space(1))) u32 gu32;

#define USE_MEMSET 0
#define WIN 4
#define STAMPS 0

constexpr int DM = 448, NH = 16, SK = 4096, DH = 256, DFF = 1792;
constexpr int AUX_NT = 2;
constexpr u32 WS_EPOCH = 0, WS_Q = 4096, WS_ML = WS_Q + 32768, WS_O = WS_ML + 4096, WS_F = WS_O + 32 * 16384,
              WS_END = WS_F + 224 * 4096, WS_STAMP = 2u << 20;
constexpr u32 SPIN_CAP = 1u << 18;

#define RLX_AGENT __ATOMIC_RELAXED, __HIP_MEMORY_SCOPE_AGENT
#define SB() __builtin_amdgcn_sched_barrier(0)

__device__ __forceinline__ rsrc_t mk(const void* p, u32 bytes) { return __builtin_amdgcn_make_buffer_rsrc((void*)p, 0, bytes, 0x00020000); }
__device__ __forceinline__ float asf(u32 x) { return __builtin_bit_cast(float, x); }
__device__ __forceinline__ u32 asu(float x) { return __builtin_bit_cast(u32, x); }
__device__ __forceinline__ float dot2(u32 a, u32 b, float c) {
  return __builtin_amdgcn_fdot2_f32_bf16(__builtin_bit_cast(bf16x2, a), __builtin_bit_cast(bf16x2, b), c, false);
}
__device__ __forceinline__ u32 pk_bf16(float lo, float hi) {
  bf16x2 r = {(__bf16)lo, (__bf16)hi};
  return __builtin_bit_cast(u32, r);
}
template <int CTRL> __device__ __forceinline__ float dpp(float x) {
  return __builtin_bit_cast(float, __builtin_amdgcn_mov_dpp(__builtin_bit_cast(int, x), CTRL, 0xf, 0xf, true));
}
constexpr int XOR1 = 0xB1, XOR2 = 0x4E, XOR7 = 0x141, ROR8 = 0x128, ROR4 = 0x124;
__device__ __forceinline__ float tr32(float a, float b) {
  auto t = __builtin_amdgcn_permlane32_swap(asu(a), asu(b), false, false);
  return asf(t[0]) + asf(t[1]);
}
__device__ __forceinline__ float tr16(float a, float b) {
  auto t = __builtin_amdgcn_permlane16_swap(asu(a), asu(b), false, false);
  return asf(t[0]) + asf(t[1]);
}
template <int CTRL> __device__ __forceinline__ float trd(float a, float b, bool hi) {
  const float s = a + dpp<CTRL>(a), t = b + dpp<CTRL>(b);
  return hi ? t : s;
}
__device__ __forceinline__ float sum8(float x) { x += dpp<XOR1>(x); x += dpp<XOR2>(x); return x + dpp<XOR7>(x); }
__device__ __forceinline__ float max8(float x) { x = fmaxf(x, dpp<XOR1>(x)); x = fmaxf(x, dpp<XOR2>(x)); return fmaxf(x, dpp<XOR7>(x)); }
__device__ __forceinline__ float sum64(float x) { x = sum8(x); x += dpp<ROR8>(x); x = tr16(x, x); return tr32(x, x); }
__device__ __forceinline__ float max64(float x) {
  x = max8(x); x = fmaxf(x, dpp<ROR8>(x));
  auto s = __builtin_amdgcn_permlane16_swap(asu(x), asu(x), false, false);
  x = fmaxf(asf(s[0]), asf(s[1]));
  auto t = __builtin_amdgcn_permlane32_swap(asu(x), asu(x), false, false);
  return fmaxf(asf(t[0]), asf(t[1]));
}
__device__ __forceinline__ void put(char* ws, u32 off, u32 tag, u32 v) { __hip_atomic_store((gu64*)(ws + off), ((u64)tag << 32) | v, RLX_AGENT); }
__device__ __forceinline__ u64 get(char* ws, u32 off) { return __hip_atomic_load((gu64*)(ws + off), RLX_AGENT); }
__device__ __forceinline__ void lds_barrier() {
  asm volatile("s_waitcnt lgkmcnt(0)" ::: "memory");
  __builtin_amdgcn_s_barrier();
  asm volatile("" ::: "memory");
}
template <int N> __device__ __forceinline__ void pace() { asm volatile("s_waitcnt vmcnt(%0)" ::"n"(N)); }

template <int N> __device__ __forceinline__ void settle(char* ws, u32 off, u32 stride, u32 tag, u64 (&g)[N]) {
  for (u32 spins = 0;; ++spins) {
    bool ok = true;
#pragma unroll
    for (int k = 0; k < N; ++k) ok &= (u32)(g[k] >> 32) == tag;
    if (__all(ok)) return;
    if (spins > SPIN_CAP) {
#pragma unroll
      for (int k = 0; k < N; ++k) g[k] = 0x7fc07fc0u;
      return;
    }
    __builtin_amdgcn_s_sleep(1);
#pragma unroll
    for (int k = 0; k < N; ++k) g[k] = get(ws, off + (k & 1) * 8 + (k >> 1) * stride);
  }
}

struct Lds {
  float opart[8][256];
  float qpart[8][32];
  float hpart[8][16];
  float mw[8], lw[8], fw[8];
  float gate, pad0;
  u32 tag, pad1;
  u32 q[128];
  unsigned short p[8][64];
};

#define STAMP(i)

__global__ void __launch_bounds__(512, 2)
fused_decode(const char* __restrict__ x, const char* __restrict__ kc, const char* __restrict__ vc, const char* __restrict__ wq,
             const char* __restrict__ wo, const char* __restrict__ w1, const char* __restrict__ w2, const char* __restrict__ gw,
             char* __restrict__ out, char* ws) {
  __shared__ __attribute__((aligned(16))) Lds S;
  const int tid = threadIdx.x, lane = tid & 63, w = __builtin_amdgcn_readfirstlane(tid >> 6);
  const int bid = blockIdx.x;
  const int h = (bid & 7) * 2 + ((bid >> 3) & 1), idx16 = bid >> 4, b = idx16 >> 3, s = idx16 & 7;
  const int bh = b * NH + h, fi = h * 7 + s;
  const bool ffn = s < 7;
  STAMP(0);

  const size_t kvoff = ((size_t)bh * SK + s * 512) * 512 + w * 4096;
  const rsrc_t rk = mk(kc + kvoff, 262144 - w * 4096), rv = mk(vc + kvoff, 262144 - w * 4096);
  const int vk = (lane >> 3) * 512 + (lane & 7) * 16, vv = lane * 16;

  u32x4 a0, a1, c0, c1;
  u32 x0, x1;
  if (w < 7) {
    const int r0 = 64 * w + (lane & ~1);
    const rsrc_t rq = mk(wq, DM * NH * DH * 2), r1 = mk(w1, ffn ? DM * DFF * 2 : 0), rx = mk(x, 2 * DM * 2);
    const int oq = r0 * 8192 + h * 512 + idx16 * 32 + (lane & 1) * 16, o1 = r0 * (DFF * 2) + fi * 32 + (lane & 1) * 16;
    a0 = __builtin_amdgcn_raw_buffer_load_b128(rq, oq, 0, 0);
    a1 = __builtin_amdgcn_raw_buffer_load_b128(rq, oq, 8192, 0);
    x0 = __builtin_amdgcn_raw_buffer_load_b32(rx, r0 * 2, 0, 0);
    x1 = __builtin_amdgcn_raw_buffer_load_b32(rx, r0 * 2, DM * 2, 0);
    c0 = __builtin_amdgcn_raw_buffer_load_b128(r1, o1, 0, 0);
    c1 = __builtin_amdgcn_raw_buffer_load_b128(r1, o1, DFF * 2, 0);
  } else {
    const rsrc_t rx = mk(x + b * DM * 2, DM * 2), rg = mk(gw, DM * 2);
    a0 = __builtin_amdgcn_raw_buffer_load_b128(rx, vv, 0, 0);
    a1 = __builtin_amdgcn_raw_buffer_load_b128(rg, vv, 0, 0);
    x0 = USE_MEMSET ? 0u : __hip_atomic_load((gu32*)(ws + WS_EPOCH), RLX_AGENT);
    x1 = 0;
    c0 = a0; c1 = a0;
  }
  SB();
  u32x4 K[32], V[WIN];
#define LDK(i) __builtin_amdgcn_raw_buffer_load_b128(rk, vk + ((i) & 3) * 128, ((i) >> 2) * 32768, AUX_NT)
#define LDV(i) __builtin_amdgcn_raw_buffer_load_b128(rv, vv + ((i) & 3) * 1024, ((i) >> 2) * 32768, AUX_NT)
#pragma unroll
  for (int i = 0; i < WIN; ++i) { K[i] = LDK(i); SB(); }

  if (w < 7) {
    float y[8], z[8];
    const u32 xb = b ? x1 : x0;
#pragma unroll
    for (int j = 0; j < 8; ++j) {
      const u32 sel = (j & 1) ? 0x07060302u : 0x05040100u;
      const u32 pq = __builtin_amdgcn_perm(a1[j >> 1], a0[j >> 1], sel);
      y[j] = tr32(dot2(pq, x0, 0.f), dot2(pq, x1, 0.f));
      z[j] = dot2(__builtin_amdgcn_perm(c1[j >> 1], c0[j >> 1], sel), xb, 0.f);
    }
#pragma unroll
    for (int j = 0; j < 4; ++j) { y[j] = tr16(y[j], y[j + 4]); z[j] = tr32(z[j], z[j + 4]); }
#pragma unroll
    for (int j = 0; j < 2; ++j) {
      y[j] = trd<XOR2>(y[j], y[j + 2], lane & 2);
      y[j] += dpp<ROR8>(y[j]); y[j] += dpp<ROR4>(y[j]);
      z[j] = tr16(z[j], z[j + 2]);
      z[j] += dpp<XOR2>(z[j]); z[j] += dpp<ROR8>(z[j]); z[j] += dpp<ROR4>(z[j]);
    }
    if ((lane & 12) == 0) *(f32x2*)&S.qpart[w][(lane >> 5) * 16 + (lane & 1) * 8 + ((lane >> 4) & 1) * 4 + (lane & 2)] = f32x2{y[0], y[1]};
    if ((lane & 14) == 0) *(f32x2*)&S.hpart[w][(lane & 1) * 8 + (lane >> 5) * 4 + ((lane >> 4) & 1) * 2] = f32x2{z[0], z[1]};
  } else {
    float d = 0.f;
#pragma unroll
    for (int j = 0; j < 4; ++j) d = dot2(a0[j], a1[j], d);
    d = sum64(d);
    if (lane == 0) { S.gate = 1.f / (1.f + __expf(-d)); S.tag = x0 + 1u; }
  }
  lds_barrier();
  const u32 tag = __builtin_amdgcn_readfirstlane(S.tag);
  if (w == 0 && lane < 16) {
    f32x2 t = {0.f, 0.f};
#pragma unroll
    for (int j = 0; j < 7; ++j) t += *(const f32x2*)&S.qpart[j][2 * lane];
    put(ws, WS_Q + (h * 2 + (lane >> 3)) * 1024 + (idx16 * 8 + (lane & 7)) * 8, tag, pk_bf16(t[0], t[1]));
  }
  STAMP(1);
  SB();

  constexpr int QI = 18;
#pragma unroll
  for (int i = WIN; i < QI; ++i) { pace<WIN - 1>(); SB(); K[i] = LDK(i); SB(); }
  u64 gq[2];
  const u32 oqg = WS_Q + (h * 2 + b) * 1024 + lane * 16;
  if (w == 1) { gq[0] = get(ws, oqg); gq[1] = get(ws, oqg + 8); }
  SB();
#pragma unroll
  for (int i = QI; i < QI + WIN; ++i) { pace<WIN - 1>(); SB(); K[i] = LDK(i); SB(); }
  if (w == 1) {
    settle<2>(ws, oqg, 0, tag, gq);
    *(u32x2*)&S.q[2 * lane] = u32x2{(u32)gq[0], (u32)gq[1]};
  }
  STAMP(2);
  SB();
#pragma unroll
  for (int i = QI + WIN; i < 32; ++i) { pace<WIN - 1>(); SB(); K[i] = LDK(i); SB(); }
#pragma unroll
  for (int i = 0; i < WIN; ++i) { pace<WIN - 1>(); SB(); V[i] = LDV(i); SB(); }
  lds_barrier();
  STAMP(3);

  {
    u32x4 q[4];
#pragma unroll
    for (int u = 0; u < 4; ++u) q[u] = *(const u32x4*)&S.q[32 * u + 4 * (lane & 7)];
    float sc[8];
#pragma unroll
    for (int t = 0; t < 8; ++t) {
      float d = 0.f;
#pragma unroll
      for (int u = 0; u < 4; ++u)
#pragma unroll
        for (int r = 0; r < 4; ++r) d = dot2(K[4 * t + u][r], q[u][r], d);
      sc[t] = d;
    }
#pragma unroll
    for (int j = 0; j < 4; ++j) sc[j] = trd<XOR7>(sc[j], sc[j + 4], lane & 4);
#pragma unroll
    for (int j = 0; j < 2; ++j) sc[j] = trd<XOR2>(sc[j], sc[j + 2], lane & 2);
    const float s2 = trd<XOR1>(sc[0], sc[1], lane & 1) * (0.0625f * 1.4426950408889634f);
    const float m = max64(s2);
    const u32 pb = pk_bf16(__builtin_amdgcn_exp2f(s2 - m), 0.f);
    S.p[w][8 * (lane & 7) + (lane >> 3)] = (unsigned short)pb;
    const float l = sum64(asf(pb << 16));
    if (lane == 0) { S.mw[w] = m; S.lw[w] = l; }
  }
  STAMP(4);

  u32 sel[4];
  {
    const int i = lane & 15, g = lane >> 4, kk = g >> 1;
    const u32 two = (u32)((2 * kk + 1) << 8 | (2 * kk));
    const u32 act = (i & 1) ? (two << 16 | 0x0c0cu) : (0x0c0c0000u | two);
#pragma unroll
    for (int r = 0; r < 4; ++r) sel[r] = ((i >> 3) == (g & 1) && r == ((i & 7) >> 1)) ? act : 0x0c0c0c0cu;
  }
  const rsrc_t ro = mk(wo + (size_t)(h * DH + 32 * s) * (DM * 2), w < 7 ? 32 * DM * 2 : 0);
  const rsrc_t r2 = mk(w2 + (size_t)(ffn ? fi : 0) * 16 * (DM * 2), (ffn && w < 7) ? 16 * DM * 2 : 0);
  const int vo = (lane >> 3) * 4 * (DM * 2) + w * 128 + (lane & 7) * 16, v2 = (lane >> 3) * 2 * (DM * 2) + w * 128 + (lane & 7) * 16;
  u32x4 WO[4], W2[2];
  f32x4 acc = {0.f, 0.f, 0.f, 0.f};
  u32x4 pp;
  u64 gm[2];
  const u32 oml = WS_ML + bh * 128 + (lane & 7) * 16;
  constexpr int MI = 20;
  asm volatile("s_waitcnt lgkmcnt(0)" ::: "memory");
#pragma unroll
  for (int i = 0; i < 32; ++i) {
    if ((i & 3) == 0) pp = *(const u32x4*)&S.p[w][2 * i];
    const u32 pr = pp[i & 3];
    const u32x4 A = {__builtin_amdgcn_perm(pr, pr, sel[0]), __builtin_amdgcn_perm(pr, pr, sel[1]), __builtin_amdgcn_perm(pr, pr, sel[2]),
                     __builtin_amdgcn_perm(pr, pr, sel[3])};
    acc = __builtin_amdgcn_mfma_f32_16x16x32_bf16(__builtin_bit_cast(bf16x8, A), __builtin_bit_cast(bf16x8, V[i % WIN]), acc, 0, 0, 0);
    SB();
    if (i + WIN < 32) V[i % WIN] = LDV(i + WIN);
    else {
      const int e = i + WIN - 32;
      if (e == 0) { W2[0] = __builtin_amdgcn_raw_buffer_load_b128(r2, v2, 0, 0); W2[1] = __builtin_amdgcn_raw_buffer_load_b128(r2, v2 + DM * 2, 0, 0); }
      if (e == 1) { WO[0] = __builtin_amdgcn_raw_buffer_load_b128(ro, vo, 0, 0); WO[1] = __builtin_amdgcn_raw_buffer_load_b128(ro, vo + DM * 2, 0, 0); }
      if (e == 2) { WO[2] = __builtin_amdgcn_raw_buffer_load_b128(ro, vo + 2 * DM * 2, 0, 0); WO[3] = __builtin_amdgcn_raw_buffer_load_b128(ro, vo + 3 * DM * 2, 0, 0); }
    }
    SB();
    if (i == 1) {
      lds_barrier();
      if (w == 7) {
        const float mv = S.mw[lane & 7], M = max8(mv), L = sum8(S.lw[lane & 7] * __builtin_amdgcn_exp2f(mv - M));
        if (lane < 2) put(ws, WS_ML + bh * 128 + s * 16 + lane * 8, tag, asu(lane ? L : M));
      }
      SB();
    }
    if (i == MI) { if (w == 7) { gm[0] = get(ws, oml); gm[1] = get(ws, oml + 8); } SB(); }
    if (i == MI + WIN) {
      if (w == 7) {
        settle<2>(ws, oml, 0, tag, gm);
        const float ms = asf((u32)gm[0]), M = max8(ms), L = sum8(asf((u32)gm[1]) * __builtin_amdgcn_exp2f(ms - M));
        if (lane < 8) S.fw[lane] = S.gate * __builtin_amdgcn_exp2f(S.mw[lane] - M) / L;
      }
      SB();
    }
  }
  STAMP(5);
  *(f32x4*)&S.opart[w][128 * (lane >> 5) + 8 * (lane & 15) + 4 * ((lane >> 4) & 1)] = acc;
  lds_barrier();
  if (w < 4) {
    const f32x4 f0 = *(const f32x4*)&S.fw[0], f1 = *(const f32x4*)&S.fw[4];
    float o = 0.f;
#pragma unroll
    for (int j = 0; j < 8; ++j) o = fmaf(j < 4 ? f0[j & 3] : f1[j & 3], S.opart[j][tid], o);
    put(ws, WS_O + (bh * 8 + (tid >> 5)) * 2048 + ((tid & 31) >> 2) * 256 + s * 32 + (tid & 3) * 8, tag, asu(o));
  }
  STAMP(6);

  if (w < 7) {
    const u32 oog = WS_O + (bh * 8 + s) * 2048 + lane * 32;
    u64 go[4];
#pragma unroll
    for (int k = 0; k < 4; ++k) go[k] = get(ws, oog + k * 8);
    float r[8];
    {
      f32x2 hv = {0.f, 0.f};
#pragma unroll
      for (int j = 0; j < 7; ++j) hv += *(const f32x2*)&S.hpart[j][2 * (lane >> 3)];
      const float og = 1.f - S.gate, h0 = fmaxf(hv[0], 0.f) * og, h1 = fmaxf(hv[1], 0.f) * og;
#pragma unroll
      for (int j = 0; j < 4; ++j) {
        r[2 * j] = fmaf(h1, asf(W2[1][j] << 16), h0 * asf(W2[0][j] << 16));
        r[2 * j + 1] = fmaf(h1, asf(W2[1][j] & 0xffff0000u), h0 * asf(W2[0][j] & 0xffff0000u));
      }
    }
    float wf[4][8];
#pragma unroll
    for (int i = 0; i < 4; ++i)
#pragma unroll
      for (int j = 0; j < 4; ++j) { wf[i][2 * j] = asf(WO[i][j] << 16); wf[i][2 * j + 1] = asf(WO[i][j] & 0xffff0000u); }
    settle<4>(ws, oog, 16, tag, go);
    STAMP(7);
#pragma unroll
    for (int i = 0; i < 4; ++i) {
      const float o = sum8(asf((u32)go[i]));
#pragma unroll
      for (int j = 0; j < 8; ++j) r[j] = fmaf(o, wf[i][j], r[j]);
    }
#pragma unroll
    for (int j = 0; j < 4; ++j) r[j] = tr32(r[j], r[j + 4]);
#pragma unroll
    for (int j = 0; j < 2; ++j) r[j] = tr16(r[j], r[j + 2]);
    const float rr = trd<ROR8>(r[0], r[1], lane & 8);
    const int cg = w * 16 + (lane >> 4) * 4 + ((lane & 7) >> 1);
    put(ws, WS_F + (b * 112 + cg) * 4096 + (h * 8 + s) * 32 + (lane & 1) * 16 + ((lane >> 3) & 1) * 8, tag, asu(rr));
    STAMP(8);
  } else if (ffn) {
    const u32 ofg = WS_F + (b * 112 + fi) * 4096 + lane * 16;
    u64 gf[8];
#pragma unroll
    for (int k = 0; k < 8; ++k) gf[k] = get(ws, ofg + (k & 1) * 8 + (k >> 1) * 1024);
    settle<8>(ws, ofg, 1024, tag, gf);
    STAMP(7);
    float e0 = (asf((u32)gf[0]) + asf((u32)gf[2])) + (asf((u32)gf[4]) + asf((u32)gf[6]));
    float e1 = (asf((u32)gf[1]) + asf((u32)gf[3])) + (asf((u32)gf[5]) + asf((u32)gf[7]));
    e0 = tr32(e0, e0); e1 = tr32(e1, e1);
    e0 = tr16(e0, e0); e1 = tr16(e1, e1);
    e0 += dpp<ROR8>(e0); e1 += dpp<ROR8>(e1);
    e0 += dpp<ROR4>(e0); e1 += dpp<ROR4>(e1);
    e0 += dpp<XOR2>(e0); e1 += dpp<XOR2>(e1);
    const int n0 = 64 * (fi >> 4) + 8 * (2 * (fi & 3) + lane) + 4 * ((fi >> 3) & 1) + 2 * ((fi >> 2) & 1);
    if (lane < 2) *(u32*)(out + (b * DM + n0) * 2) = pk_bf16(e0, e1);
    if (!USE_MEMSET && bid == 0 && lane == 0) __hip_atomic_store((gu32*)(ws + WS_EPOCH), tag, RLX_AGENT);
    STAMP(8);
  }
}

extern "C" void kernel_launch(void* const* d_in, const int* in_sizes, int n_in, void* d_out, int out_size, void* d_ws, size_t ws_size,
                              hipStream_t stream) {
  if (USE_MEMSET) (void)hipMemsetAsync(d_ws, 0, WS_END, stream);
  fused_decode<<<256, 512, 0, stream>>>((const char*)d_in[0], (const char*)d_in[1], (const char*)d_in[2], (const char*)d_in[3],
                                        (const char*)d_in[4], (const char*)d_in[6], (const char*)d_in[7], (const char*)d_in[8],
                                        (char*)d_out, (char*)d_ws);
}
```

```cpp
#include <hip/hip_runtime.h>
#include <hip/hip_bf16.h>

typedef unsigned u32;
typedef unsigned long long u64;
typedef u32 u32x2 __attribute__((ext_vector_type(2)));
typedef u32 u32x4 __attribute__((ext_vector_type(4)));
typedef float f32x2 __attribute__((ext_vector_type(2)));
typedef float f32x4 __attribute__((ext_vector_type(4)));
typedef __bf16 bf16x2 __attribute__((ext_vector_type(2)));
typedef __bf16 bf16x8 __attribute__((ext_vector_type(8)));
typedef __amdgpu_buffer_rsrc_t rsrc_t;
typedef __attribute__((address_space(1))) u64 gu64;
typedef __attribute__((address_space(1))) u32 gu32;

#define USE_MEMSET 0
#define WIN 4
#define OSLEEP 2
#define FSLEEP0 8
#define FSLEEP 2
#define STAMPS 0

constexpr int DM = 448, NH = 16, SK = 4096, DH = 256, DFF = 1792;
#define AUXK 2
#define AUXV 2
#define AUXW 2
constexpr u32 WS_EPOCH = 0, WS_Q = 4096, WS_H = WS_Q + 32 * 8192, WS_ML = WS_H + 224 * 1024, WS_O = WS_ML + 4096,
              WS_F = WS_O + 32 * 16384, WS_END = WS_F + 224 * 4096, WS_STAMP = 4u << 20;
constexpr u32 SPIN_CAP = 1u << 18;

#define RLX_AGENT __ATOMIC_RELAXED, __HIP_MEMORY_SCOPE_AGENT
#define SB() __builtin_amdgcn_sched_barrier(0)

__device__ __forceinline__ rsrc_t mk(const void* p, u32 bytes) { return __builtin_amdgcn_make_buffer_rsrc((void*)p, 0, bytes, 0x00020000); }
__device__ __forceinline__ float asf(u32 x) { return __builtin_bit_cast(float, x); }
__device__ __forceinline__ u32 asu(float x) { return __builtin_bit_cast(u32, x); }
__device__ __forceinline__ float dot2(u32 a, u32 b, float c) {
  return __builtin_amdgcn_fdot2_f32_bf16(__builtin_bit_cast(bf16x2, a), __builtin_bit_cast(bf16x2, b), c, false);
}
__device__ __forceinline__ u32 pk_bf16(float lo, float hi) {
  bf16x2 r = {(__bf16)lo, (__bf16)hi};
  return __builtin_bit_cast(u32, r);
}
template <int CTRL> __device__ __forceinline__ float dpp(float x) {
  return __builtin_bit_cast(float, __builtin_amdgcn_mov_dpp(__builtin_bit_cast(int, x), CTRL, 0xf, 0xf, true));
}
constexpr int XOR1 = 0xB1, XOR2 = 0x4E, XOR7 = 0x141, ROR8 = 0x128, ROR4 = 0x124;
__device__ __forceinline__ float tr32(float a, float b) {
  auto t = __builtin_amdgcn_permlane32_swap(asu(a), asu(b), false, false);
  return asf(t[0]) + asf(t[1]);
}
__device__ __forceinline__ float tr16(float a, float b) {
  auto t = __builtin_amdgcn_permlane16_swap(asu(a), asu(b), false, false);
  return asf(t[0]) + asf(t[1]);
}
template <int CTRL> __device__ __forceinline__ float trd(float a, float b, bool hi) {
  const float s = a + dpp<CTRL>(a), t = b + dpp<CTRL>(b);
  return hi ? t : s;
}
__device__ __forceinline__ float sum8(float x) { x += dpp<XOR1>(x); x += dpp<XOR2>(x); return x + dpp<XOR7>(x); }
__device__ __forceinline__ float max8(float x) { x = fmaxf(x, dpp<XOR1>(x)); x = fmaxf(x, dpp<XOR2>(x)); return fmaxf(x, dpp<XOR7>(x)); }
__device__ __forceinline__ float sum64(float x) { x = sum8(x); x += dpp<ROR8>(x); x = tr16(x, x); return tr32(x, x); }
__device__ __forceinline__ float max64(float x) {
  x = max8(x); x = fmaxf(x, dpp<ROR8>(x));
  auto s = __builtin_amdgcn_permlane16_swap(asu(x), asu(x), false, false);
  x = fmaxf(asf(s[0]), asf(s[1]));
  auto t = __builtin_amdgcn_permlane32_swap(asu(x), asu(x), false, false);
  return fmaxf(asf(t[0]), asf(t[1]));
}
__device__ __forceinline__ void put(char* ws, u32 off, u32 tag, u32 v) { __hip_atomic_store((gu64*)(ws + off), ((u64)tag << 32) | v, RLX_AGENT); }
__device__ __forceinline__ u64 get(char* ws, u32 off) { return __hip_atomic_load((gu64*)(ws + off), RLX_AGENT); }
__device__ __forceinline__ void lds_barrier() {
  asm volatile("s_waitcnt lgkmcnt(0)" ::: "memory");
  __builtin_amdgcn_s_barrier();
  asm volatile("" ::: "memory");
}
template <int N> __device__ __forceinline__ void pace() { asm volatile("s_waitcnt vmcnt(%0)" ::"n"(N)); }

template <int N, int SLEEP> __device__ __forceinline__ void settle(char* ws, u32 off, u32 stride, u32 tag, u64 (&g)[N]) {
  bool ok = true;
#pragma unroll
  for (int k = 0; k < N; ++k) ok &= (u32)(g[k] >> 32) == tag;
  if (__builtin_expect(!__all(ok), 0)) {
    u32 spins = 0;
#pragma clang loop unroll(disable)
    do {
      __builtin_amdgcn_s_sleep(SLEEP);
      if (!ok) {
#pragma unroll
        for (int k = 0; k < N; ++k) g[k] = get(ws, off + k * stride);
        ok = true;
#pragma unroll
        for (int k = 0; k < N; ++k) ok &= (u32)(g[k] >> 32) == tag;
      }
    } while (!__all(ok) && ++spins < SPIN_CAP);
    if (!ok) {
#pragma unroll
      for (int k = 0; k < N; ++k) g[k] = 0x7fc07fc0u;
    }
  }
}

struct Lds {
  float opart[8][256];
  float part[7][256];
  float hs[16], os[32];
  float mw[8], lw[8], fw[8];
  float gate, pad0;
  u32 tag, pad1;
  unsigned short q[256];
  unsigned short p[8][64];
};

#define STAMP(i)

__global__ void __launch_bounds__(512, 2)
fused_decode(const char* __restrict__ x, const char* __restrict__ kc, const char* __restrict__ vc, const char* __restrict__ wq,
             const char* __restrict__ wo, const char* __restrict__ w1, const char* __restrict__ w2, const char* __restrict__ gw,
             char* __restrict__ out, char* ws) {
  __shared__ __attribute__((aligned(16))) Lds S;
  const int tid = threadIdx.x, lane = tid & 63, w = __builtin_amdgcn_readfirstlane(tid >> 6);
  const int bid = blockIdx.x;
  const int h = (bid & 7) * 2 + ((bid >> 3) & 1), idx16 = bid >> 4, b = idx16 >> 3, s = idx16 & 7;
  const int bh = b * NH + h, fi = h * 7 + s;
  const bool ffn = s < 7;
  STAMP(0);

  const size_t kvoff = ((size_t)bh * SK + s * 512) * 512 + w * 4096;
  const rsrc_t rk = mk(kc + kvoff, 262144 - w * 4096), rv = mk(vc + kvoff, 262144 - w * 4096);
  const int vk = (lane >> 3) * 512 + (lane & 7) * 16, vv = lane * 16;

  const int f2 = b * 112 + fi;
  u32x4 a0, a1, c0;
  u32 x0, x1, z0, z1;
  if (w < 7) {
    const int r0 = 112 * (idx16 >> 2) + 16 * w + 2 * (lane >> 3), r1 = 56 * (f2 & 7) + 8 * w + (lane >> 3);
    const rsrc_t rq = mk(wq, DM * NH * DH * 2), rw = mk(w1, ffn ? DM * DFF * 2 : 0), rx = mk(x, 2 * DM * 2);
    const int oq = r0 * 8192 + h * 512 + (idx16 & 3) * 128 + (lane & 7) * 16, o1 = r1 * (DFF * 2) + (f2 >> 3) * 128 + (lane & 7) * 16;
    a0 = __builtin_amdgcn_raw_buffer_load_b128(rq, oq, 0, AUXW);
    a1 = __builtin_amdgcn_raw_buffer_load_b128(rq, oq, 8192, AUXW);
    c0 = __builtin_amdgcn_raw_buffer_load_b128(rw, o1, 0, AUXW);
    x0 = __builtin_amdgcn_raw_buffer_load_b32(rx, r0 * 2, 0, 0);
    x1 = __builtin_amdgcn_raw_buffer_load_b32(rx, r0 * 2, DM * 2, 0);
    z0 = __builtin_amdgcn_raw_buffer_load_b16(rx, r1 * 2, 0, 0);
    z1 = __builtin_amdgcn_raw_buffer_load_b16(rx, r1 * 2, DM * 2, 0);
  } else {
    const rsrc_t rx = mk(x + b * DM * 2, DM * 2), rg = mk(gw, DM * 2);
    a0 = __builtin_amdgcn_raw_buffer_load_b128(rx, vv, 0, 0);
    a1 = __builtin_amdgcn_raw_buffer_load_b128(rg, vv, 0, 0);
    x0 = USE_MEMSET ? 0u : __hip_atomic_load((gu32*)(ws + WS_EPOCH), RLX_AGENT);
    x1 = 0; z0 = 0; z1 = 0;
    c0 = u32x4{0u, 0u, 0u, 0u};
  }
  SB();
  u32x4 K[32], V[WIN];
#define LDK(i) __builtin_amdgcn_raw_buffer_load_b128(rk, vk + ((i) & 3) * 128, ((i) >> 2) * 32768, AUXK)
#define LDV(i) __builtin_amdgcn_raw_buffer_load_b128(rv, vv + ((i) & 3) * 1024, ((i) >> 2) * 32768, AUXV)
#pragma unroll
  for (int i = 0; i < WIN; ++i) { K[i] = LDK(i); SB(); }

  if (w < 7) {
    float y[8], z[8];
    asm volatile("" : "+v"(z0), "+v"(z1));
    const float zf0 = asf(z0 << 16), zf1 = asf(z1 << 16);
#pragma unroll
    for (int j = 0; j < 8; ++j) {
      const u32 pq = __builtin_amdgcn_perm(a1[j >> 1], a0[j >> 1], (j & 1) ? 0x07060302u : 0x05040100u);
      y[j] = tr32(dot2(pq, x0, 0.f), dot2(pq, x1, 0.f));
      const float c = (j & 1) ? asf(c0[j >> 1] & 0xffff0000u) : asf(c0[j >> 1] << 16);
      z[j] = tr32(c * zf0, c * zf1);
    }
#pragma unroll
    for (int j = 0; j < 4; ++j) { y[j] = tr16(y[j], y[j + 4]); z[j] = tr16(z[j], z[j + 4]); }
#pragma unroll
    for (int j = 0; j < 2; ++j) { y[j] = trd<ROR8>(y[j], y[j + 2], lane & 8); z[j] = trd<ROR8>(z[j], z[j + 2], lane & 8); }
    const int e = (lane >> 5) * 64 + (lane & 7) * 8 + ((lane >> 4) & 1) * 4 + ((lane >> 3) & 1) * 2;
    *(f32x2*)&S.part[w][e] = f32x2{y[0], y[1]};
    *(f32x2*)&S.part[w][128 + e] = f32x2{z[0], z[1]};
  } else {
    float d = 0.f;
#pragma unroll
    for (int j = 0; j < 4; ++j) d = dot2(a0[j], a1[j], d);
    d = sum64(d);
    if (lane == 0) { S.gate = 1.f / (1.f + __expf(-d)); S.tag = x0 + 1u; }
  }
  SB();
#pragma unroll
  for (int i = WIN; i < WIN + 2; ++i) { pace<WIN - 1>(); SB(); K[i] = LDK(i); SB(); }
  lds_barrier();
  const u32 tag = __builtin_amdgcn_readfirstlane(S.tag);
  if (w < (ffn ? 4 : 2)) {
    float t = 0.f;
#pragma unroll
    for (int j = 0; j < 7; ++j) t += S.part[j][tid];
    const u32 dq = WS_Q + (h * 2 + w) * 8192 + (idx16 >> 2) * 2048 + (idx16 & 3) * 512 + lane * 8;
    const u32 dh = WS_H + ((w & 1) * 112 + (f2 >> 3) * 4 + (lane >> 4)) * 1024 + (f2 & 7) * 128 + (lane & 15) * 8;
    put(ws, w < 2 ? dq : dh, tag, asu(t));
  }
  STAMP(1);
  SB();

  constexpr int QI = 20;
#pragma unroll
  for (int i = WIN + 2; i < QI; ++i) { pace<WIN - 1>(); SB(); K[i] = LDK(i); SB(); }
  u64 gq[4], gh[2];
  const u32 oqg = WS_Q + (h * 2 + b) * 8192 + tid * 8, ohg = WS_H + f2 * 1024 + lane * 8;
  if (w < 4) {
#pragma unroll
    for (int k = 0; k < 4; ++k) gq[k] = get(ws, oqg + k * 2048);
  } else if (w == 4 && ffn) {
    gh[0] = get(ws, ohg); gh[1] = get(ws, ohg + 512);
  }
  SB();
#pragma unroll
  for (int i = QI; i < QI + WIN; ++i) { if (i > QI) pace<WIN - 1>(); SB(); K[i] = LDK(i); SB(); }
  if (w < 4) {
    settle<4, 8>(ws, oqg, 2048, tag, gq);
    S.q[tid] = (unsigned short)pk_bf16((asf((u32)gq[0]) + asf((u32)gq[1])) + (asf((u32)gq[2]) + asf((u32)gq[3])), 0.f);
  } else if (w == 4) {
    float t = 0.f;
    if (ffn) {
      settle<2, 8>(ws, ohg, 512, tag, gh);
      t = asf((u32)gh[0]) + asf((u32)gh[1]);
      t = tr16(t, t); t = tr32(t, t);
      t = (t < 0.f ? 0.f : t) * (1.f - S.gate);
    }
    if (lane < 16) S.hs[lane] = t;
  }
  STAMP(2);
  SB();
#pragma unroll
  for (int i = QI + WIN; i < 32; ++i) { pace<WIN - 1>(); SB(); K[i] = LDK(i); SB(); }
#pragma unroll
  for (int i = 0; i < WIN; ++i) { pace<WIN - 1>(); SB(); V[i] = LDV(i); SB(); }
  lds_barrier();
  STAMP(3);

  {
    u32x4 q[4];
#pragma unroll
    for (int u = 0; u < 4; ++u) q[u] = *(const u32x4*)&S.q[64 * u + 8 * (lane & 7)];
    float sc[8];
#pragma unroll
    for (int t = 0; t < 8; ++t) {
      float d = 0.f;
#pragma unroll
      for (int u = 0; u < 4; ++u)
#pragma unroll
        for (int r = 0; r < 4; ++r) d = dot2(K[4 * t + u][r], q[u][r], d);
      sc[t] = d;
    }
#pragma unroll
    for (int j = 0; j < 4; ++j) sc[j] = trd<XOR7>(sc[j], sc[j + 4], lane & 4);
#pragma unroll
    for (int j = 0; j < 2; ++j) sc[j] = trd<XOR2>(sc[j], sc[j + 2], lane & 2);
    const float s2 = trd<XOR1>(sc[0], sc[1], lane & 1) * (0.0625f * 1.4426950408889634f);
    const float m = max64(s2);
    const u32 pb = pk_bf16(__builtin_amdgcn_exp2f(s2 - m), 0.f);
    S.p[w][8 * (lane & 7) + (lane >> 3)] = (unsigned short)pb;
    const float l = sum64(asf(pb << 16));
    if (lane == 0) { S.mw[w] = m; S.lw[w] = l; }
  }
  STAMP(4);

  u32 sel[4];
  {
    const int i = lane & 15, g = lane >> 4, kk = g >> 1;
    const u32 two = (u32)((2 * kk + 1) << 8 | (2 * kk));
    const u32 act = (i & 1) ? (two << 16 | 0x0c0cu) : (0x0c0c0000u | two);
#pragma unroll
    for (int r = 0; r < 4; ++r) sel[r] = ((i >> 3) == (g & 1) && r == ((i & 7) >> 1)) ? act : 0x0c0c0c0cu;
  }
  const rsrc_t ro = mk(wo + (size_t)(h * DH + 32 * s) * (DM * 2), w < 7 ? 32 * DM * 2 : 0);
  const rsrc_t r2 = mk(w2 + (size_t)(ffn ? fi : 0) * 16 * (DM * 2), (ffn && w < 7) ? 16 * DM * 2 : 0);
  const int vo = (lane >> 3) * 4 * (DM * 2) + w * 128 + (lane & 7) * 16, v2 = (lane >> 3) * 2 * (DM * 2) + w * 128 + (lane & 7) * 16;
  u32x4 WO[4], W2[2];
  f32x4 acc = {0.f, 0.f, 0.f, 0.f};
  u32x4 pp;
  u64 gm[2];
  const u32 oml = WS_ML + bh * 128 + (lane & 7) * 16;
  constexpr int MI = 20;
  asm volatile("s_waitcnt lgkmcnt(0)" ::: "memory");
#pragma unroll
  for (int i = 0; i < 32; ++i) {
    if ((i & 3) == 0) pp = *(const u32x4*)&S.p[w][2 * i];
    const u32 pr = pp[i & 3];
    const u32x4 A = {__builtin_amdgcn_perm(pr, pr, sel[0]), __builtin_amdgcn_perm(pr, pr, sel[1]), __builtin_amdgcn_perm(pr, pr, sel[2]),
                     __builtin_amdgcn_perm(pr, pr, sel[3])};
    acc = __builtin_amdgcn_mfma_f32_16x16x32_bf16(__builtin_bit_cast(bf16x8, A), __builtin_bit_cast(bf16x8, V[i % WIN]), acc, 0, 0, 0);
    SB();
    if (i + WIN < 32) V[i % WIN] = LDV(i + WIN);
    else {
      const int e = i + WIN - 32;
      if (e == 0) { W2[0] = __builtin_amdgcn_raw_buffer_load_b128(r2, v2, 0, 0); W2[1] = __builtin_amdgcn_raw_buffer_load_b128(r2, v2 + DM * 2, 0, 0); }
      if (e == 1) { WO[0] = __builtin_amdgcn_raw_buffer_load_b128(ro, vo, 0, 0); WO[1] = __builtin_amdgcn_raw_buffer_load_b128(ro, vo + DM * 2, 0, 0); }
      if (e == 2) { WO[2] = __builtin_amdgcn_raw_buffer_load_b128(ro, vo + 2 * DM * 2, 0, 0); WO[3] = __builtin_amdgcn_raw_buffer_load_b128(ro, vo + 3 * DM * 2, 0, 0); }
    }
    SB();
    if (i == 1) {
      lds_barrier();
      if (w == 7) {
        const float mv = S.mw[lane & 7], M = max8(mv), L = sum8(S.lw[lane & 7] * __builtin_amdgcn_exp2f(mv - M));
        if (lane < 2) put(ws, WS_ML + bh * 128 + s * 16 + lane * 8, tag, asu(lane ? L : M));
      }
      SB();
    }
    if (i == MI) { if (w == 7) { gm[0] = get(ws, oml); gm[1] = get(ws, oml + 8); } SB(); }
    if (i == MI + WIN) {
      if (w == 7) {
        settle<2, 8>(ws, oml, 8, tag, gm);
        const float ms = asf((u32)gm[0]), M = max8(ms), L = sum8(asf((u32)gm[1]) * __builtin_amdgcn_exp2f(ms - M));
        if (lane < 8) S.fw[lane] = S.gate * __builtin_amdgcn_exp2f(S.mw[lane] - M) / L;
      }
      SB();
    }
  }
  STAMP(5);
  *(f32x4*)&S.opart[w][128 * (lane >> 5) + 8 * (lane & 15) + 4 * ((lane >> 4) & 1)] = acc;
  lds_barrier();
  if (w < 4) {
    const f32x4 f0 = *(const f32x4*)&S.fw[0], f1 = *(const f32x4*)&S.fw[4];
    float o = 0.f;
#pragma unroll
    for (int j = 0; j < 8; ++j) o = fmaf(j < 4 ? f0[j & 3] : f1[j & 3], S.opart[j][tid], o);
    put(ws, WS_O + (bh * 8 + (tid >> 5)) * 2048 + s * 256 + (tid & 31) * 8, tag, asu(o));
  }
  STAMP(6);

  if (w < 7) {
    float r[8], wf[4][8];
    const f32x2 hv = *(const f32x2*)&S.hs[2 * (lane >> 3)];
#pragma unroll
    for (int j = 0; j < 4; ++j) {
      r[2 * j] = fmaf(hv[1], asf(W2[1][j] << 16), hv[0] * asf(W2[0][j] << 16));
      r[2 * j + 1] = fmaf(hv[1], asf(W2[1][j] & 0xffff0000u), hv[0] * asf(W2[0][j] & 0xffff0000u));
    }
#pragma unroll
    for (int i = 0; i < 4; ++i)
#pragma unroll
      for (int j = 0; j < 4; ++j) { wf[i][2 * j] = asf(WO[i][j] << 16); wf[i][2 * j + 1] = asf(WO[i][j] & 0xffff0000u); }
#pragma unroll
    for (int j = 0; j < 8; ++j) asm volatile("" : "+v"(r[j]), "+v"(wf[0][j]), "+v"(wf[1][j]), "+v"(wf[2][j]), "+v"(wf[3][j]));
    lds_barrier();
    STAMP(7);
    const f32x4 o = *(const f32x4*)&S.os[4 * (lane >> 3)];
#pragma unroll
    for (int i = 0; i < 4; ++i)
#pragma unroll
      for (int j = 0; j < 8; ++j) r[j] = fmaf(o[i], wf[i][j], r[j]);
#pragma unroll
    for (int j = 0; j < 4; ++j) r[j] = tr32(r[j], r[j + 4]);
#pragma unroll
    for (int j = 0; j < 2; ++j) r[j] = tr16(r[j], r[j + 2]);
    const float rr = trd<ROR8>(r[0], r[1], lane & 8);
    const int cg = w * 16 + (lane & 7) * 2 + (lane >> 5);
    put(ws, WS_F + (b * 112 + cg) * 4096 + (h * 8 + s) * 32 + ((lane >> 3) & 3) * 8, tag, asu(rr));
    STAMP(8);
  } else {
    const u32 oog = WS_O + (bh * 8 + s) * 2048 + lane * 8;
    u64 go[4];
#pragma unroll
    for (int k = 0; k < 4; ++k) go[k] = get(ws, oog + k * 512);
    settle<4, OSLEEP>(ws, oog, 512, tag, go);
    float o = (asf((u32)go[0]) + asf((u32)go[1])) + (asf((u32)go[2]) + asf((u32)go[3]));
    o = tr32(o, o);
    if (lane < 32) S.os[lane] = o;
    lds_barrier();
    if (ffn) {
      const u32 ofg = WS_F + f2 * 4096 + lane * 8;
      u64 gf[8];
      __builtin_amdgcn_s_sleep(FSLEEP0);
#pragma unroll
      for (int k = 0; k < 8; ++k) gf[k] = get(ws, ofg + k * 512);
      settle<8, FSLEEP>(ws, ofg, 512, tag, gf);
      STAMP(7);
      float e = ((asf((u32)gf[0]) + asf((u32)gf[1])) + (asf((u32)gf[2]) + asf((u32)gf[3]))) +
                ((asf((u32)gf[4]) + asf((u32)gf[5])) + (asf((u32)gf[6]) + asf((u32)gf[7])));
      e = tr32(e, e); e = tr16(e, e);
      e += dpp<ROR8>(e); e += dpp<ROR4>(e);
      const u32 pk = pk_bf16(e, dpp<XOR1>(e));
      if ((lane & ~2) == 0) __hip_atomic_store((gu32*)(out + (b * DM + 4 * fi + lane) * 2), pk, RLX_AGENT);
      if (!USE_MEMSET && bid == 0 && lane == 0) __hip_atomic_store((gu32*)(ws + WS_EPOCH), tag, RLX_AGENT);
      STAMP(8);
    }
  }
}

extern "C" void kernel_launch(void* const* d_in, const int* in_sizes, int n_in, void* d_out, int out_size, void* d_ws, size_t ws_size,
                              hipStream_t stream) {
  if (USE_MEMSET) (void)hipMemsetAsync(d_ws, 0, WS_END, stream);
  fused_decode<<<256, 512, 0, stream>>>((const char*)d_in[0], (const char*)d_in[1], (const char*)d_in[2], (const char*)d_in[3],
                                        (const char*)d_in[4], (const char*)d_in[6], (const char*)d_in[7], (const char*)d_in[8],
                                        (char*)d_out, (char*)d_ws);
}
```

```cpp
#include <hip/hip_runtime.h>
#include <hip/hip_bf16.h>

typedef unsigned u32;
typedef unsigned long long u64;
typedef u32 u32x2 __attribute__((ext_vector_type(2)));
typedef u32 u32x4 __attribute__((ext_vector_type(4)));
typedef float f32x2 __attribute__((ext_vector_type(2)));
typedef float f32x4 __attribute__((ext_vector_type(4)));
typedef __bf16 bf16x2 __attribute__((ext_vector_type(2)));
typedef __bf16 bf16x8 __attribute__((ext_vector_type(8)));
typedef __amdgpu_buffer_rsrc_t rsrc_t;
typedef __attribute__((address_space(1))) u32 gu32;

constexpr int DM = 448, NH = 16, SK = 4096, DH = 256, DFF = 1792;
constexpr int NT = 2, SC1 = 16;
constexpr u32 WS_EPOCH = 0, WS_Q = 4096, WS_H = WS_Q + 32 * 8192, WS_ML = WS_H + 224 * 1024, WS_O = WS_ML + 4096,
              WS_T = WS_O + 32 * 16384, WS_F = WS_T + 112 * 1024, WS_C = WS_F + 224 * 4096, WS_END = WS_C + 8192;
constexpr u32 SPIN_CAP = 1u << 18, NOWHERE = 0x80000000u, QNAN = 0x7fc07fc0u;

constexpr u32 MAP_I[5] = {658, 544, 64, 256, 944}, MAP_W[3] = {45, 117, 552}, MAP_S[3] = {48, 1194, 353};
constexpr u32 mapi(int i) { u32 r = 0; for (int k = 0; k < 5; ++k) if ((i >> k) & 1) r ^= MAP_I[k]; return r * 1024; }

constexpr int WIN = 6, WIN0 = 4;
constexpr int PUB = 5, CGET = 9, WARM = 12, CUSE = 13;
constexpr int QGET = 17, QUSE = QGET + 1 + WIN, QB = QUSE + 1;
constexpr int SM = 31 + WIN, MB = SM + 3;
constexpr int WAT = 44, WN = 3, WLATE = 4;
constexpr int extra(int i) {
  return i == PUB ? 2 : i == QGET ? 4 : (i == CGET || i == WARM || i == MB || (i >= WAT && i < WAT + WN)) ? 1 : 0;
}
constexpr int younger(int i, int win) { int n = win - 1; for (int j = i - win; j < i; ++j) n += extra(j); return n; }
constexpr int score_step(int t) { return QB + 1 + t > 4 * t + 3 + WIN ? QB + 1 + t : 4 * t + 3 + WIN; }
constexpr u32 due(int i) { return (1250 + 264 * i + 238 * (i > WAT + WN ? WN : i > WAT ? i - WAT : 0)) / 10; }
constexpr int CLEAD = 28, FLAG = 248;

#define RLX_AGENT __ATOMIC_RELAXED, __HIP_MEMORY_SCOPE_AGENT
#define RLX_GROUP __ATOMIC_RELAXED, __HIP_MEMORY_SCOPE_WORKGROUP
#define SB() __builtin_amdgcn_sched_barrier(0)

__device__ __forceinline__ rsrc_t mk(const void* p, u32 bytes) { return __builtin_amdgcn_make_buffer_rsrc((void*)p, 0, bytes, 0x00020000); }
__device__ __forceinline__ float asf(u32 x) { return __builtin_bit_cast(float, x); }
__device__ __forceinline__ u32 asu(float x) { return __builtin_bit_cast(u32, x); }
__device__ __forceinline__ float dot2(u32 a, u32 b, float c) {
  return __builtin_amdgcn_fdot2_f32_bf16(__builtin_bit_cast(bf16x2, a), __builtin_bit_cast(bf16x2, b), c, false);
}
__device__ __forceinline__ u32 pk_bf16(float lo, float hi) {
  bf16x2 r = {(__bf16)lo, (__bf16)hi};
  return __builtin_bit_cast(u32, r);
}
template <int CTRL> __device__ __forceinline__ float dpp(float x) {
  return __builtin_bit_cast(float, __builtin_amdgcn_mov_dpp(__builtin_bit_cast(int, x), CTRL, 0xf, 0xf, true));
}
constexpr int XOR1 = 0xB1, XOR2 = 0x4E, XOR7 = 0x141, ROR8 = 0x128, ROR4 = 0x124;
__device__ __forceinline__ float tr32(float a, float b) {
  auto t = __builtin_amdgcn_permlane32_swap(asu(a), asu(b), false, false);
  return asf(t[0]) + asf(t[1]);
}
__device__ __forceinline__ float tr16(float a, float b) {
  auto t = __builtin_amdgcn_permlane16_swap(asu(a), asu(b), false, false);
  return asf(t[0]) + asf(t[1]);
}
template <int CTRL> __device__ __forceinline__ float trd(float a, float b, bool hi) {
  const float s = a + dpp<CTRL>(a), t = b + dpp<CTRL>(b);
  return hi ? t : s;
}
__device__ __forceinline__ float sum8(float x) { x += dpp<XOR1>(x); x += dpp<XOR2>(x); return x + dpp<XOR7>(x); }
__device__ __forceinline__ float max8(float x) { x = fmaxf(x, dpp<XOR1>(x)); x = fmaxf(x, dpp<XOR2>(x)); return fmaxf(x, dpp<XOR7>(x)); }
__device__ __forceinline__ float sum64(float x) { x = sum8(x); x += dpp<ROR8>(x); x = tr16(x, x); return tr32(x, x); }
__device__ __forceinline__ float max64(float x) {
  x = max8(x); x = fmaxf(x, dpp<ROR8>(x));
  auto s = __builtin_amdgcn_permlane16_swap(asu(x), asu(x), false, false);
  x = fmaxf(asf(s[0]), asf(s[1]));
  auto t = __builtin_amdgcn_permlane32_swap(asu(x), asu(x), false, false);
  return fmaxf(asf(t[0]), asf(t[1]));
}
__device__ __forceinline__ void put(rsrc_t r, u32 off, u32 tag, u32 v) { __builtin_amdgcn_raw_buffer_store_b64(u32x2{v, tag}, r, off, 0, SC1); }
__device__ __forceinline__ u32x2 get(rsrc_t r, u32 off) { return __builtin_amdgcn_raw_buffer_load_b64(r, off, 0, SC1); }
__device__ __forceinline__ u32 now() { return (u32)__builtin_amdgcn_s_memrealtime(); }
template <int I> struct Idx { static constexpr int v = I; };
template <int A, int B, class F> __device__ __forceinline__ void steps(F&& f) {
  if constexpr (A < B) { f(Idx<A>{}); steps<A + 1, B>(f); }
}
__device__ __forceinline__ void lds_barrier() {
  asm volatile("s_waitcnt lgkmcnt(0)" ::: "memory");
  __builtin_amdgcn_s_barrier();
  asm volatile("" ::: "memory");
}
template <int N> __device__ __forceinline__ void pace() { asm volatile("s_waitcnt vmcnt(%0)" ::"n"(N)); }

#define STEER_LEVEL(t, n) "s_cmpk_ge_u32 vcc_lo, %" #t "\n\ts_cbranch_scc1 1f\n\ts_waitcnt vmcnt(%" #n ")\n\t"
template <int I> __device__ __forceinline__ void steer(u32 t0) {
  asm volatile("s_memrealtime vcc\n\ts_waitcnt lgkmcnt(0)\n\ts_sub_u32 vcc_lo, vcc_lo, %0\n\t"
               STEER_LEVEL(1, 2) STEER_LEVEL(3, 4) STEER_LEVEL(5, 6) "\n1:"
               :: "s"(t0), "n"(due(I) + 30), "n"(younger(I, 4)), "n"(due(I)), "n"(younger(I, 3)), "n"(due(I) - 40), "n"(younger(I, 2))
               : "vcc", "scc");
}

template <int N, int SLEEP> __device__ __forceinline__ void settle(rsrc_t r, u32 off, u32 stride, u32 tag, u32x2 (&g)[N]) {
  bool ok = true;
#pragma unroll
  for (int k = 0; k < N; ++k) ok &= g[k][1] == tag;
  if (__builtin_expect(!__all(ok), 0)) {
    u32 spins = 0;
#pragma clang loop unroll(disable)
    do {
      __builtin_amdgcn_s_sleep(SLEEP);
      asm volatile("" ::: "memory");
      if (!ok) {
#pragma unroll
        for (int k = 0; k < N; ++k) g[k] = get(r, off + k * stride);
        ok = true;
#pragma unroll
        for (int k = 0; k < N; ++k) ok &= g[k][1] == tag;
      }
    } while (!__all(ok) && ++spins < SPIN_CAP);
    if (!ok) {
#pragma unroll
      for (int k = 0; k < N; ++k) g[k][0] = QNAN;
    }
  }
}

__device__ __forceinline__ void take(u32x2 g, u32 tag, u32& off, u32& val) {
  const bool hit = (g[1] == tag) & ((int)off >= 0);
  val = hit ? g[0] : val;
  off = hit ? NOWHERE : off;
}

struct Lds {
  float opart[8][256];
  float part[7][256];
  float hs[16], os[32];
  float mw[8], lw[8];
  float gate;
  u32 tag, fire, t0;
  unsigned short q[256];
  unsigned short p[8][64];
};

__global__ void __launch_bounds__(512, 2)
fused_decode(const char* x, const char* kc, const char* vc, const char* wq, const char* wo, const char* w1, const char* w2, const char* gw,
             char* out, char* ws) {
  __shared__ __attribute__((aligned(16))) Lds S;
  u64 pc;
  u32 span;
  asm volatile("s_getpc_b64 %0\n.Lcode_head:\n\ts_mov_b32 %1, .Lcode_tail-.Lcode_head" : "=s"(pc), "=s"(span));
  const u32 code = __builtin_amdgcn_raw_buffer_load_b32(mk((const void*)pc, span),
                                                        (threadIdx.x & 31) ? NOWHERE : (blockIdx.x & 56) * 256 + threadIdx.x * 4, 0, 0);
  SB();
  asm volatile("" : "+s"(x), "+s"(kc), "+s"(vc), "+s"(wq), "+s"(wo), "+s"(w1), "+s"(w2), "+s"(gw), "+s"(out), "+s"(ws));
  const int tid = threadIdx.x, lane = tid & 63, w = __builtin_amdgcn_readfirstlane(tid >> 6);
  const int bid = blockIdx.x;
  const int h = (bid & 7) * 2 + ((bid >> 3) & 1), idx16 = bid >> 4, b = idx16 >> 3, s = idx16 & 7;
  const int bh = b * NH + h, fi = h * 7 + s, f2 = b * 112 + fi;
  const bool ffn = s < 7;
  const u32 tstart = now();
  u32 t0 = tstart;

  u32 kvo = 0;
#pragma unroll
  for (int k = 0; k < 3; ++k) kvo ^= (((w >> k) & 1) * MAP_W[k] ^ ((s >> k) & 1) * MAP_S[k]) * 1024;
  const size_t kvoff = (size_t)bh * SK * 512;
  const rsrc_t rk = mk(kc + kvoff, SK * 512), rv = mk(vc + kvoff, SK * 512), rws = mk(ws, WS_END);
  const int vv = lane * 16;

  u32x4 a0, a1, c0;
  u32 x0, x1, z0, z1;
  if (w < 7) {
    const int r0 = 112 * (idx16 >> 2) + 16 * w + 2 * (lane >> 3), r1 = 56 * (f2 & 7) + 8 * w + (lane >> 3);
    const rsrc_t rq = mk(wq, DM * NH * DH * 2), rw = mk(w1, ffn ? DM * DFF * 2 : 0), rx = mk(x, 2 * DM * 2);
    const int oq = r0 * 8192 + h * 512 + (idx16 & 3) * 128 + (lane & 7) * 16, o1 = r1 * (DFF * 2) + (f2 >> 3) * 128 + (lane & 7) * 16;
    a0 = __builtin_amdgcn_raw_buffer_load_b128(rq, oq, 0, NT);
    a1 = __builtin_amdgcn_raw_buffer_load_b128(rq, oq, 8192, NT);
    c0 = __builtin_amdgcn_raw_buffer_load_b128(rw, o1, 0, NT);
    x0 = __builtin_amdgcn_raw_buffer_load_b32(rx, r0 * 2, 0, 0);
    x1 = __builtin_amdgcn_raw_buffer_load_b32(rx, r0 * 2, DM * 2, 0);
    z0 = __builtin_amdgcn_raw_buffer_load_b16(rx, r1 * 2, 0, 0);
    z1 = __builtin_amdgcn_raw_buffer_load_b16(rx, r1 * 2, DM * 2, 0);
  } else {
    const rsrc_t rx = mk(x + b * DM * 2, DM * 2), rg = mk(gw, DM * 2);
    a0 = __builtin_amdgcn_raw_buffer_load_b128(rx, vv, 0, 0);
    a1 = __builtin_amdgcn_raw_buffer_load_b128(rg, vv, 0, 0);
    x0 = __hip_atomic_load((gu32*)(ws + WS_EPOCH), RLX_AGENT);
    x1 = 0; z0 = 0; z1 = 0;
    c0 = u32x4{0u, 0u, 0u, 0u};
  }
  SB();

  u32x4 K[32], V[32], q, pp, A;
  u32x2 gq[2], gh[2], gc;
  u32 tag, sel[4], warm;
  float sc[8], s2, m, fw;
  f32x4 acc = {0.f, 0.f, 0.f, 0.f};
  const u32 oqg = WS_Q + (h * 2 + b) * 8192 + (lane >> 5) * 2048 + (w * 32 + (lane & 31)) * 8, ohg = WS_H + f2 * 1024 + lane * 8;
  const u32 opub = (w < 4 && (tid >> 5) != s) ? WS_O + (bh * 8 + (tid >> 5)) * 2048 + s * 256 + (tid & 31) * 8 : NOWHERE;
  const u32 fpub = WS_F + (b * 112 + w * 16 + (lane & 7) * 2 + (lane >> 5)) * 4096 + (h * 8 + s) * 32 + ((lane >> 3) & 3) * 8;

  const rsrc_t ro = mk(wo + (size_t)(h * DH + 32 * s) * (DM * 2), w < 7 ? 32 * DM * 2 : 0);
  const rsrc_t r2 = mk(w2 + (size_t)(ffn ? fi : 0) * 16 * (DM * 2), (ffn && w < 7) ? 16 * DM * 2 : 0);
  const int vo = (lane >> 3) * 4 * (DM * 2) + w * 128 + (lane & 7) * 16, v2 = (lane >> 3) * 2 * (DM * 2) + w * 128 + (lane & 7) * 16;
  u32x4 WO[4], W2[2];
  auto ldw = [&](int j) __attribute__((always_inline)) {
    if (j < 2) W2[j] = __builtin_amdgcn_raw_buffer_load_b128(r2, v2 + j * DM * 2, 0, NT);
    else WO[j - 2] = __builtin_amdgcn_raw_buffer_load_b128(ro, vo + (j - 2) * DM * 2, 0, NT);
  };

  auto build_a = [&](int j) __attribute__((always_inline)) {
    if ((j & 3) == 0) pp = *(const u32x4*)&S.p[w][2 * j];
    const u32 pr = pp[j & 3];
    A = u32x4{__builtin_amdgcn_perm(pr, pr, sel[0]), __builtin_amdgcn_perm(pr, pr, sel[1]), __builtin_amdgcn_perm(pr, pr, sel[2]),
              __builtin_amdgcn_perm(pr, pr, sel[3])};
  };
  auto pv = [&](int j) __attribute__((always_inline)) {
    acc = __builtin_amdgcn_mfma_f32_16x16x32_bf16(__builtin_bit_cast(bf16x8, A), __builtin_bit_cast(bf16x8, V[j]), acc, 0, 0, 0);
    if (j < 31) build_a(j + 1);
  };

  steps<0, 64>([&](auto I) __attribute__((always_inline)) {
    constexpr int i = I.v;
    if constexpr (i >= WIN0) pace<younger(i, i <= CUSE + 1 ? WIN0 : WIN)>();
    if constexpr (i > PUB) steer<i>(t0);
    SB();
    if constexpr (i < 32) K[i] = __builtin_amdgcn_raw_buffer_load_b128(rk, vv, kvo ^ mapi(i), NT);
    else V[i - 32] = __builtin_amdgcn_raw_buffer_load_b128(rv, vv, kvo ^ mapi(i & 31), NT);
    SB();

    if constexpr (i == 63) {
      put(rws, (w == 6 && lane < 14) ? WS_T + (b * 56 + lane) * 1024 + (h * 8 + s) * 8 : NOWHERE, tag, now());
      SB();
    }
    if constexpr (i >= WAT && i < WAT + WN) { ldw(i - WAT); SB(); }
    if constexpr (i == WARM) {
      const u32 ti = (lane < 48 && ffn && (fi & 7) == 0) ? WS_T + (b * 56 + (fi >> 3)) * 1024 + (lane - 32) * 64 : NOWHERE;
      warm = __builtin_amdgcn_raw_buffer_load_b32(rws, w < 7 ? NOWHERE : lane < 32 ? WS_O + (bh * 8 + s) * 2048 + lane * 64 : ti, 0, SC1);
      SB();
    }
    if constexpr (i == WARM + WIN + 1) { asm volatile("" :: "v"(warm)); SB(); }
    if constexpr (i == 3) {
      if (w < 7) {
        float y[8], z[8];
        asm volatile("" : "+v"(z0), "+v"(z1));
        const float zf0 = asf(z0 << 16), zf1 = asf(z1 << 16);
#pragma unroll
        for (int j = 0; j < 8; ++j) {
          const u32 pq = __builtin_amdgcn_perm(a1[j >> 1], a0[j >> 1], (j & 1) ? 0x07060302u : 0x05040100u);
          y[j] = tr32(dot2(pq, x0, 0.f), dot2(pq, x1, 0.f));
          const float c = (j & 1) ? asf(c0[j >> 1] & 0xffff0000u) : asf(c0[j >> 1] << 16);
          z[j] = tr32(c * zf0, c * zf1);
        }
#pragma unroll
        for (int j = 0; j < 4; ++j) { y[j] = tr16(y[j], y[j + 4]); z[j] = tr16(z[j], z[j + 4]); }
#pragma unroll
        for (int j = 0; j < 2; ++j) { y[j] = trd<ROR8>(y[j], y[j + 2], lane & 8); z[j] = trd<ROR8>(z[j], z[j + 2], lane & 8); }
        const int e = (lane >> 5) * 64 + (lane & 7) * 8 + ((lane >> 4) & 1) * 4 + ((lane >> 3) & 1) * 2;
        *(f32x2*)&S.part[w][e] = f32x2{y[0], y[1]};
        *(f32x2*)&S.part[w][128 + e] = f32x2{z[0], z[1]};
      } else {
        float d = 0.f;
#pragma unroll
        for (int j = 0; j < 4; ++j) d = dot2(a0[j], a1[j], d);
        d = sum64(d);
        if (lane == 0) { S.gate = 1.f / (1.f + __expf(-d)); S.tag = x0 * 0x9E3779B1u + 0x7F4A7C15u; S.t0 = tstart; }
      }
    }
    if constexpr (i == PUB) {
      asm volatile("" :: "v"(code));
      lds_barrier();
      tag = __builtin_amdgcn_readfirstlane(S.tag);
      float t = 0.f;
#pragma unroll
      for (int j = 0; j < 7; ++j) t += S.part[j][tid & 255];
      const u32 dq = WS_Q + (h * 2 + w) * 8192 + (idx16 >> 2) * 2048 + (idx16 & 3) * 512 + lane * 8;
      const u32 dh = WS_H + ((w & 1) * 112 + (f2 >> 3) * 4 + (lane >> 4)) * 1024 + (f2 & 7) * 128 + (lane & 15) * 8;
      put(rws, w < 2 ? dq : (ffn && w < 4) ? dh : NOWHERE, tag, asu(t));
      put(rws, (w == 6 && (bid & 24) == 0 && lane < 16) ? WS_C + lane * 512 + ((bid >> 5) * 8 + (bid & 7)) * 8 : NOWHERE, tag, now());
    }
    if constexpr (i == CGET) gc = get(rws, w == 6 ? WS_C + idx16 * 512 + lane * 8 : NOWHERE);
    if constexpr (i == CUSE) {
      if (w == 6) {
        const bool ok = gc[1] == tag;
        const float n = sum64(ok ? 1.f : 0.f), d = sum64(ok ? (float)(int)(gc[0] - tstart) : 0.f);
        if (lane == 0 && n >= 16.f) __hip_atomic_store(&S.t0, tstart + (int)(d * __builtin_amdgcn_rcpf(n)) + CLEAD - due(PUB), RLX_GROUP);
      }
    }
    if constexpr (i == CUSE || i == CUSE + 1 || i == CUSE + 3) t0 = __builtin_amdgcn_readfirstlane(__hip_atomic_load(&S.t0, RLX_GROUP));
    if constexpr (i == QGET) {
      const u32 oh = (w == 4 && ffn) ? ohg : NOWHERE;
      gq[0] = get(rws, oqg); gq[1] = get(rws, oqg + 4096);
      gh[0] = get(rws, oh); gh[1] = get(rws, oh + 512);
    }
    if constexpr (i == QUSE) {
      settle<2, 8>(rws, oqg, 4096, tag, gq);
      float t = asf(gq[0][0]) + asf(gq[1][0]);
      t = tr32(t, t);
      if (lane < 32) S.q[w * 32 + lane] = (unsigned short)pk_bf16(t, 0.f);
      if (w == 4) {
        if (ffn) settle<2, 8>(rws, ohg, 512, tag, gh);
        float u = asf(gh[0][0]) + asf(gh[1][0]);
        u = tr16(u, u); u = tr32(u, u);
        if (lane < 16) S.hs[lane] = (u < 0.f ? 0.f : u) * (1.f - S.gate);
      }
    }
    if constexpr (i == QB) {
      lds_barrier();
      q = *(const u32x4*)&S.q[8 * (lane & 31)];
    }
    steps<0, 8>([&](auto T) __attribute__((always_inline)) {
      constexpr int t = T.v;
      if constexpr (i == score_step(t)) {
        float d[4];
#pragma unroll
        for (int u = 0; u < 4; ++u) {
          d[u] = 0.f;
#pragma unroll
          for (int r = 0; r < 4; ++r) d[u] = dot2(K[4 * t + u][r], q[r], d[u]);
        }
        sc[t] = trd<ROR8>(tr16(d[0], d[1]), tr16(d[2], d[3]), lane & 8);
        if constexpr (t & 1) sc[t] = trd<XOR7>(sc[t - 1], sc[t], lane & 4);
        if constexpr ((t & 3) == 3) sc[t] = trd<XOR2>(sc[t - 2], sc[t], lane & 2);
        asm volatile("" : "+v"(sc[t]));
      }
    });
    if constexpr (i == SM) {
      s2 = trd<XOR1>(sc[3], sc[7], lane & 1) * (0.0625f * 1.4426950408889634f);
      m = max64(s2);
      asm volatile("" : "+v"(s2), "+v"(m));
    }
    if constexpr (i == SM + 1) {
      const u32 pb = pk_bf16(__builtin_amdgcn_exp2f(s2 - m), 0.f);
      S.p[w][__builtin_bitreverse32(lane) >> 26] = (unsigned short)pb;
      const float l = sum64(asf(pb << 16));
      if (lane == 0) { S.mw[w] = m; S.lw[w] = l; }
      const int ii = lane & 15, g = lane >> 4, kk = g >> 1;
      const u32 two = (u32)((2 * kk + 1) << 8 | (2 * kk));
      const u32 act = (ii & 1) ? (two << 16 | 0x0c0cu) : (0x0c0c0000u | two);
#pragma unroll
      for (int r = 0; r < 4; ++r) sel[r] = ((ii >> 3) == (g & 1) && r == ((ii & 7) >> 1)) ? act : 0x0c0c0c0cu;
    }
    if constexpr (i == SM + 2) {
      asm volatile("s_waitcnt lgkmcnt(0)" ::: "memory");
      build_a(0);
      pv(0);
    }
    if constexpr (i >= SM + 2) pv(i - 32 - WIN);
    if constexpr (i == MB) {
      lds_barrier();
      const float mv = S.mw[lane & 7], M = max8(mv), L = sum8(S.lw[lane & 7] * __builtin_amdgcn_exp2f(mv - M));
      put(rws, (w == 7 && lane < 2) ? WS_ML + bh * 128 + s * 16 + lane * 8 : NOWHERE, tag, asu(lane ? L : M));
      fw = __builtin_amdgcn_exp2f(m - M);
      asm volatile("" : "+v"(fw));
    }
    SB();
  });

  const u32 oml = WS_ML + bh * 128 + (lane & 7) * 16;
  u32x2 gm[2];
  steps<0, WIN>([&](auto E) __attribute__((always_inline)) {
    constexpr int e = E.v;
    pv(32 - WIN + e);
    SB();
    if constexpr (e == 0) {
      gm[0] = get(rws, w == 7 ? oml : NOWHERE); gm[1] = get(rws, w == 7 ? oml + 8 : NOWHERE);
    }
    if constexpr (e == WLATE) {
#pragma unroll
      for (int j = WN; j < 6; ++j) ldw(j);
    }
    SB();
  });
  *(f32x4*)&S.opart[w][128 * (lane >> 5) + 8 * (lane & 15) + 4 * ((lane >> 4) & 1)] = acc * fw;
  lds_barrier();

  if (w < 7) {
    u32 v;
    {
      float o[8];
#pragma unroll
      for (int j = 0; j < 8; ++j) o[j] = S.opart[j][tid & 255];
      v = asu(((o[0] + o[1]) + (o[2] + o[3])) + ((o[4] + o[5]) + (o[6] + o[7])));
    }
    put(rws, opub, tag, v);
    const u32 ot = (w == 6 && ffn) ? WS_T + (b * 56 + (fi >> 3)) * 1024 + lane * 8 : NOWHERE;
    u32x2 gt[2];
    gt[0] = get(rws, ot); gt[1] = get(rws, ot + 512);
    asm volatile("" ::: "memory");
    SB();
    float r[8], wf[4][8];
    const f32x2 hv = *(const f32x2*)&S.hs[2 * (lane >> 3)];
#pragma unroll
    for (int j = 0; j < 4; ++j) {
      r[2 * j] = fmaf(hv[1], asf(W2[1][j] << 16), hv[0] * asf(W2[0][j] << 16));
      r[2 * j + 1] = fmaf(hv[1], asf(W2[1][j] & 0xffff0000u), hv[0] * asf(W2[0][j] & 0xffff0000u));
    }
#pragma unroll
    for (int i = 0; i < 4; ++i)
#pragma unroll
      for (int j = 0; j < 4; ++j) { wf[i][2 * j] = asf(WO[i][j] << 16); wf[i][2 * j + 1] = asf(WO[i][j] & 0xffff0000u); }
#pragma unroll
    for (int j = 0; j < 8; ++j) asm volatile("" : "+v"(r[j]), "+v"(wf[0][j]), "+v"(wf[1][j]), "+v"(wf[2][j]), "+v"(wf[3][j]));
    if (w == 6 && ffn) {
      settle<2, 8>(rws, ot, 512, tag, gt);
      const u32 t = now();
      const float age = -max64(-fminf((float)(t - gt[0][0]), (float)(t - gt[1][0])));
      if (lane == 0) S.fire = t + FLAG - (u32)fminf(age, (float)FLAG);
    }
    lds_barrier();
    const f32x4 o = *(const f32x4*)&S.os[4 * (lane >> 3)];
#pragma unroll
    for (int i = 0; i < 4; ++i)
#pragma unroll
      for (int j = 0; j < 8; ++j) r[j] = fmaf(o[i], wf[i][j], r[j]);
#pragma unroll
    for (int j = 0; j < 4; ++j) r[j] = tr32(r[j], r[j + 4]);
#pragma unroll
    for (int j = 0; j < 2; ++j) r[j] = tr16(r[j], r[j + 2]);
    put(rws, fpub, tag, asu(trd<ROR8>(r[0], r[1], lane & 8)));
    asm volatile(".Lcode_tail:");
  } else {
    u32 oo[4], val[4];
    u32x2 go[4];
    float wt[4];
#pragma unroll
    for (int k = 0; k < 4; ++k) {
      oo[k] = 2 * k + (lane >> 5) == s ? NOWHERE : WS_O + (bh * 8 + s) * 2048 + k * 512 + lane * 8;
      go[k] = get(rws, oo[k]);
    }
    {
      const float* op = &S.opart[4 * (lane >> 5)][32 * s + (lane & 31)];
      float own = (op[0] + op[256]) + (op[512] + op[768]);
      own = tr32(own, own);
      settle<2, 8>(rws, oml, 8, tag, gm);
      const float ms = asf(gm[0][0]), M = max8(ms), e = __builtin_amdgcn_exp2f(ms - M), c = S.gate * e / sum8(asf(gm[1][0]) * e);
#pragma unroll
      for (int k = 0; k < 4; ++k) {
        wt[k] = asf(__builtin_amdgcn_ds_bpermute((2 * k + (lane >> 5)) * 4, asu(c)));
        val[k] = asu(own);
        asm volatile("" : "+v"(wt[k]));
      }
    }
    u32 spins = 0;
#pragma clang loop unroll(disable)
    while (true) {
#pragma unroll
      for (int k = 0; k < 4; ++k)
        take(go[k], tag, oo[k], val[k]);
      if (__all((oo[0] & oo[1] & oo[2] & oo[3]) == NOWHERE)) break;
      if (++spins == SPIN_CAP) { val[0] = QNAN; break; }
      __builtin_amdgcn_s_sleep(2);
      asm volatile("" ::: "memory");
#pragma unroll
      for (int k = 0; k < 4; ++k) go[k] = get(rws, oo[k]);
    }
    float o = fmaf(asf(val[0]), wt[0], asf(val[1]) * wt[1]) + fmaf(asf(val[2]), wt[2], asf(val[3]) * wt[3]);
    o = tr32(o, o);
    if (lane < 32) S.os[lane] = o;
    lds_barrier();
    if (ffn) {
      const u32 fire = __builtin_amdgcn_readfirstlane(S.fire);
      while (fire - now() - 1u < (u32)FLAG) {}
      asm volatile("" ::: "memory");
      const u32 ofg = WS_F + f2 * 4096 + lane * 8;
      u32x2 gf[8];
#pragma unroll
      for (int k = 0; k < 8; ++k) gf[k] = get(rws, ofg + k * 512);
      settle<8, 2>(rws, ofg, 512, tag, gf);
      float e = ((asf(gf[0][0]) + asf(gf[1][0])) + (asf(gf[2][0]) + asf(gf[3][0]))) + ((asf(gf[4][0]) + asf(gf[5][0])) + (asf(gf[6][0]) + asf(gf[7][0])));
      e = tr32(e, e); e = tr16(e, e);
      e += dpp<ROR8>(e); e += dpp<ROR4>(e);
      const u32 pk = pk_bf16(e, dpp<XOR1>(e));
      const u32 pk2 = asu(dpp<XOR2>(asf(pk)));
      __builtin_amdgcn_raw_buffer_store_b64(u32x2{pk, pk2}, mk(out, 2 * DM * 2), lane ? NOWHERE : f2 * 8, 0, SC1);
      if (bid == 0 && lane == 0) __hip_atomic_store((gu32*)(ws + WS_EPOCH), tag, RLX_AGENT);
    }
  }
}

extern "C" void kernel_launch(void* const* d_in, const int* in_sizes, int n_in, void* d_out, int out_size, void* d_ws, size_t ws_size,
                              hipStream_t stream) {
  fused_decode<<<256, 512, 0, stream>>>((const char*)d_in[0], (const char*)d_in[1], (const char*)d_in[2], (const char*)d_in[3],
                                        (const char*)d_in[4], (const char*)d_in[6], (const char*)d_in[7], (const char*)d_in[8],
                                        (char*)d_out, (char*)d_ws);
}
```

```cpp
#include <hip/hip_runtime.h>
#include <hip/hip_bf16.h>

typedef unsigned u32;
typedef unsigned long long u64;
typedef u32 u32x2 __attribute__((ext_vector_type(2)));
typedef u32 u32x4 __attribute__((ext_vector_type(4)));
typedef u64 u64x2 __attribute__((ext_vector_type(2)));
typedef u64 u64x8 __attribute__((ext_vector_type(8)));
typedef float f32x2 __attribute__((ext_vector_type(2)));
typedef float f32x4 __attribute__((ext_vector_type(4)));
typedef __bf16 bf16x2 __attribute__((ext_vector_type(2)));
typedef __bf16 bf16x8 __attribute__((ext_vector_type(8)));
typedef __amdgpu_buffer_rsrc_t rsrc_t;
typedef __attribute__((address_space(1))) u32 gu32;
typedef __attribute__((address_space(3))) float lf32;
typedef __attribute__((address_space(3))) f32x4 lf32x4;

constexpr int DM = 448, NH = 16, SK = 4096, DH = 256, DFF = 1792;
constexpr int NT = 2, SC1 = 16;
constexpr u32 WS_EPOCH = 0, WS_Q = 4096, WS_H = WS_Q + 32 * 8192, WS_ML = WS_H + 224 * 1024, WS_O = WS_ML + 4096,
              WS_T = WS_O + 32 * 16384, WS_F = WS_T + 112 * 1024, WS_C = WS_F + 224 * 4096, WS_END = WS_C + 8192;
constexpr u32 SPIN_CAP = 1u << 18, NOWHERE = 0x80000000u, QNAN = 0x7fc07fc0u;

constexpr u32 MAP_I[5] = {658, 544, 64, 256, 944}, MAP_W[3] = {45, 117, 552}, MAP_S[3] = {48, 1194, 353};
constexpr u32 mapi(int i) { u32 r = 0; for (int k = 0; k < 5; ++k) if ((i >> k) & 1) r ^= MAP_I[k]; return r * 1024; }

constexpr int WIN = 6, WIN0 = 4;
constexpr int PUB = 5, CGET = 9, CUSE = 13;
constexpr int QGET = 17, QUSE = QGET + 1 + WIN, QB = QUSE + 1;
constexpr int SM = 31 + WIN, MB = SM + 3;
constexpr int WAT = 44, WN = 3, WLATE = 4;
constexpr int extra(int i) {
  return i == PUB ? 2 : i == QGET ? 4 : (i == CGET || i == MB || (i >= WAT && i < WAT + WN)) ? 1 : 0;
}
constexpr int younger(int i, int win) { int n = win - 1; for (int j = i - win; j < i; ++j) n += extra(j); return n; }
constexpr int score_step(int t) { return QB + 1 + t > 4 * t + 3 + WIN ? QB + 1 + t : 4 * t + 3 + WIN; }
constexpr u32 due(int i) { return (1000 + 262 * i + 238 * (i > WAT + WN ? WN : i > WAT ? i - WAT : 0)) / 10; }
constexpr int CLEAD = 28;
constexpr u32 FLAG = 222;
constexpr u32 OGATE = due(63) + 140, FGATE = OGATE + 112;

#define RLX_AGENT __ATOMIC_RELAXED, __HIP_MEMORY_SCOPE_AGENT
#define RLX_GROUP __ATOMIC_RELAXED, __HIP_MEMORY_SCOPE_WORKGROUP
#define SB() __builtin_amdgcn_sched_barrier(0)

__device__ __forceinline__ rsrc_t mk(const void* p, u32 bytes) { return __builtin_amdgcn_make_buffer_rsrc((void*)p, 0, bytes, 0x00020000); }
__device__ __forceinline__ float asf(u32 x) { return __builtin_bit_cast(float, x); }
__device__ __forceinline__ u32 asu(float x) { return __builtin_bit_cast(u32, x); }
__device__ __forceinline__ float dot2(u32 a, u32 b, float c) {
  return __builtin_amdgcn_fdot2_f32_bf16(__builtin_bit_cast(bf16x2, a), __builtin_bit_cast(bf16x2, b), c, false);
}
__device__ __forceinline__ u32 pk_bf16(float lo, float hi) {
  bf16x2 r = {(__bf16)lo, (__bf16)hi};
  return __builtin_bit_cast(u32, r);
}
__device__ __forceinline__ f32x2 widen(u32 x) { return f32x2{asf(x << 16), asf(x & 0xffff0000u)}; }
template <int CTRL> __device__ __forceinline__ float dpp(float x) {
  return __builtin_bit_cast(float, __builtin_amdgcn_mov_dpp(__builtin_bit_cast(int, x), CTRL, 0xf, 0xf, true));
}
constexpr int XOR1 = 0xB1, XOR2 = 0x4E, XOR7 = 0x141, ROR8 = 0x128, ROR4 = 0x124;
__device__ __forceinline__ float tr32(float a, float b) {
  auto t = __builtin_amdgcn_permlane32_swap(asu(a), asu(b), false, false);
  return asf(t[0]) + asf(t[1]);
}
__device__ __forceinline__ float tr16(float a, float b) {
  auto t = __builtin_amdgcn_permlane16_swap(asu(a), asu(b), false, false);
  return asf(t[0]) + asf(t[1]);
}
template <int CTRL> __device__ __forceinline__ float trd(float a, float b, bool hi) {
  const float s = a + dpp<CTRL>(a), t = b + dpp<CTRL>(b);
  return hi ? t : s;
}
__device__ __forceinline__ float sum8(float x) { x += dpp<XOR1>(x); x += dpp<XOR2>(x); return x + dpp<XOR7>(x); }
__device__ __forceinline__ float max8(float x) { x = fmaxf(x, dpp<XOR1>(x)); x = fmaxf(x, dpp<XOR2>(x)); return fmaxf(x, dpp<XOR7>(x)); }
__device__ __forceinline__ float sum64(float x) { x = sum8(x); x += dpp<ROR8>(x); x = tr16(x, x); return tr32(x, x); }
__device__ __forceinline__ float max64(float x) {
  x = max8(x); x = fmaxf(x, dpp<ROR8>(x));
  auto s = __builtin_amdgcn_permlane16_swap(asu(x), asu(x), false, false);
  x = fmaxf(asf(s[0]), asf(s[1]));
  auto t = __builtin_amdgcn_permlane32_swap(asu(x), asu(x), false, false);
  return fmaxf(asf(t[0]), asf(t[1]));
}
__device__ __forceinline__ void put(rsrc_t r, u32 off, u32 tag, u32 v) { __builtin_amdgcn_raw_buffer_store_b64(u32x2{v, tag}, r, off, 0, SC1); }
__device__ __forceinline__ u32x2 get(rsrc_t r, u32 off) { return __builtin_amdgcn_raw_buffer_load_b64(r, off, 0, SC1); }
__device__ __forceinline__ u32 now() { return (u32)__builtin_amdgcn_s_memrealtime(); }
__device__ __forceinline__ void until(u32 t) {
  while (t - now() - 1u < FLAG) {}
  asm volatile("" ::: "memory");
}
template <int I> struct Idx { static constexpr int v = I; };
template <int A, int B, class F> __device__ __forceinline__ void steps(F&& f) {
  if constexpr (A < B) { f(Idx<A>{}); steps<A + 1, B>(f); }
}
__device__ __forceinline__ void lds_barrier() {
  asm volatile("s_waitcnt lgkmcnt(0)" ::: "memory");
  __builtin_amdgcn_s_barrier();
  asm volatile("" ::: "memory");
}
template <int N> __device__ __forceinline__ void pace() { asm volatile("s_waitcnt vmcnt(%0)" ::"n"(N)); }

#define STEER_LEVEL(t, n) "s_cmpk_ge_u32 vcc_lo, %" #t "\n\ts_cbranch_scc1 1f\n\ts_waitcnt vmcnt(%" #n ")\n\t"
template <int I> __device__ __forceinline__ void steer(u32 t0) {
  asm volatile("s_memrealtime vcc\n\ts_waitcnt lgkmcnt(0)\n\ts_sub_u32 vcc_lo, vcc_lo, %0\n\t"
               STEER_LEVEL(1, 2) STEER_LEVEL(3, 4) STEER_LEVEL(5, 6) "\n1:"
               :: "s"(t0), "n"(due(I) + 45), "n"(younger(I, 4)), "n"(due(I)), "n"(younger(I, 3)), "n"(due(I) - 40), "n"(younger(I, 2))
               : "vcc", "scc");
}

template <int N, int SLEEP> __device__ __forceinline__ void settle(rsrc_t r, u32 off, u32 stride, u32 tag, u32x2 (&g)[N]) {
  bool ok = true;
#pragma unroll
  for (int k = 0; k < N; ++k) ok &= g[k][1] == tag;
  if (__builtin_expect(!__all(ok), 0)) {
    u32 spins = 0;
#pragma clang loop unroll(disable)
    do {
      __builtin_amdgcn_s_sleep(SLEEP);
      asm volatile("" ::: "memory");
      if (!ok) {
#pragma unroll
        for (int k = 0; k < N; ++k) g[k] = get(r, off + k * stride);
        ok = true;
#pragma unroll
        for (int k = 0; k < N; ++k) ok &= g[k][1] == tag;
      }
    } while (!__all(ok) && ++spins < SPIN_CAP);
    if (!ok) {
#pragma unroll
      for (int k = 0; k < N; ++k) g[k][0] = QNAN;
    }
  }
}

struct Lds {
  float opart[8][256];
  float part[7][256];
  float hs[16], os[32];
  float mw[8], lw[8];
  float gate;
  u32 tag, fire, t0;
  unsigned short q[256];
  unsigned short p[8][64];
};

__global__ void __launch_bounds__(512, 2)
fused_decode(const char*, const char*, const char*, const char*, const char*, const char*, const char*, const char*, char*, char*) {
  __shared__ __attribute__((aligned(16))) Lds S;
  u64x8 ain;
  u64x2 aout;
  asm volatile("s_load_dwordx16 %0, %2, 0\n\ts_load_dwordx4 %1, %2, 64" : "=&s"(ain), "=&s"(aout) : "s"(__builtin_amdgcn_kernarg_segment_ptr()));
  u64 pc;
  u32 span;
  asm volatile("s_getpc_b64 %0\n.Lcode_head:\n\ts_mov_b32 %1, .Lcode_tail-.Lcode_head" : "=s"(pc), "=s"(span));
  const u32 code = __builtin_amdgcn_raw_buffer_load_b32(mk((const void*)pc, span),
                                                        (threadIdx.x & 31) ? NOWHERE : (blockIdx.x & 56) * 256 + threadIdx.x * 4, 0, 0);
  SB();
  const int tid = threadIdx.x, lane = tid & 63, w = __builtin_amdgcn_readfirstlane(tid >> 6);
  const int bid = blockIdx.x;
  const int h = (bid & 7) * 2 + ((bid >> 3) & 1), idx16 = bid >> 4, b = idx16 >> 3, s = idx16 & 7;
  const int bh = b * NH + h, fi = h * 7 + s, f2 = b * 112 + fi;
  const bool ffn = s < 7;

  u32 kvo = bh * (SK * 512);
#pragma unroll
  for (int k = 0; k < 3; ++k) kvo ^= (((w >> k) & 1) * MAP_W[k] ^ ((s >> k) & 1) * MAP_S[k]) * 1024;
  u32 kv1 = kvo ^ mapi(1), kv2 = kvo ^ mapi(2), kv3 = kvo ^ mapi(3), n1 = ffn ? DM * DFF * 2 : 0;
  u32 vv = lane * 16, r0 = (112 * (idx16 >> 2) + 16 * w + 2 * (lane >> 3)) * 2, r1 = (56 * (f2 & 7) + 8 * w + (lane >> 3)) * 2;
  u32 oq = r0 * 4096 + h * 512 + (idx16 & 3) * 128 + (lane & 7) * 16, o1 = r1 * DFF + (f2 >> 3) * 128 + (lane & 7) * 16;
  u32 oo = WS_O + (bh * 8 + s) * 2048 + lane * 8, ot = WS_T + (b * 56 + (fi >> 3)) * 1024 + lane * 8;
  asm volatile("" : "+v"(oo), "+v"(ot), "+s"(kvo), "+s"(kv1), "+s"(kv2), "+s"(kv3), "+s"(n1), "+v"(vv), "+v"(r0), "+v"(r1), "+v"(oq), "+v"(o1));
  SB();
  asm volatile("s_waitcnt lgkmcnt(0)" : "+s"(ain), "+s"(aout));
  const char *x = (const char*)ain[0], *kc = (const char*)ain[1], *vc = (const char*)ain[2], *wq = (const char*)ain[3],
             *wo = (const char*)ain[4], *w1 = (const char*)ain[5], *w2 = (const char*)ain[6], *gw = (const char*)ain[7];
  char *out = (char*)aout[0], *ws = (char*)aout[1];
  const u32 tstart = now();
  u32 t0 = tstart;
  const rsrc_t rk = mk(kc, 32 * SK * 512), rv = mk(vc, 32 * SK * 512), rws = mk(ws, WS_END);

  u32x4 a0, a1, c0;
  u32 x0, x1, z0, z1;
  if (w < 7) {
    const rsrc_t rq = mk(wq, DM * NH * DH * 2), rw = mk(w1, n1), rx = mk(x, 2 * DM * 2);
    a0 = __builtin_amdgcn_raw_buffer_load_b128(rq, oq, 0, NT);
    a1 = __builtin_amdgcn_raw_buffer_load_b128(rq, oq, 8192, NT);
    c0 = __builtin_amdgcn_raw_buffer_load_b128(rw, o1, 0, NT);
    x0 = __builtin_amdgcn_raw_buffer_load_b32(rx, r0, 0, 0);
    x1 = __builtin_amdgcn_raw_buffer_load_b32(rx, r0, DM * 2, 0);
    z0 = __builtin_amdgcn_raw_buffer_load_b16(rx, r1, 0, 0);
    z1 = __builtin_amdgcn_raw_buffer_load_b16(rx, r1, DM * 2, 0);
  } else {
    const rsrc_t rx = mk(x + b * DM * 2, DM * 2), rg = mk(gw, DM * 2);
    a0 = __builtin_amdgcn_raw_buffer_load_b128(rx, vv, 0, 0);
    a1 = __builtin_amdgcn_raw_buffer_load_b128(rg, vv, 0, 0);
    x0 = __hip_atomic_load((gu32*)(ws + WS_EPOCH), RLX_AGENT);
#pragma unroll
    for (int k = 0; k < 4; ++k) c0[k] = __builtin_amdgcn_raw_buffer_load_b32(rws, oo + k * 512 + 4, 0, SC1);
    x1 = __builtin_amdgcn_raw_buffer_load_b32(rws, (lane < 16 && ffn && (fi & 7) == 0) ? ot + lane * 56 : NOWHERE, 0, SC1);
    z0 = 0; z1 = 0;
  }
  SB();

  u32x4 K[32], V[32], q, pp, A;
  K[0] = __builtin_amdgcn_raw_buffer_load_b128(rk, vv, kvo, NT);
  SB();
  K[1] = __builtin_amdgcn_raw_buffer_load_b128(rk, vv, kv1, NT);
  SB();
  K[2] = __builtin_amdgcn_raw_buffer_load_b128(rk, vv, kv2, NT);
  SB();
  K[3] = __builtin_amdgcn_raw_buffer_load_b128(rk, vv, kv3, NT);
  SB();
  u32x2 gq[2], gh[2], gc;
  u32 tag, sel[4];
  float sc[8], s2, m, fw;
  f32x4 acc = {0.f, 0.f, 0.f, 0.f};
  const u32 oqg = WS_Q + (h * 2 + b) * 8192 + (lane >> 5) * 2048 + (w * 32 + (lane & 31)) * 8, ohg = WS_H + f2 * 1024 + lane * 8;
  u32 opub = (w < 4 && (tid >> 5) != s) ? WS_O + (bh * 8 + (tid >> 5)) * 2048 + s * 256 + (tid & 31) * 8 : NOWHERE;
  const u32 fpub = WS_F + (b * 112 + w * 16 + (lane & 7) * 2 + (lane >> 5)) * 4096 + (h * 8 + s) * 32 + ((lane >> 3) & 3) * 8;

  const rsrc_t ro = mk(wo + (size_t)(h * DH + 32 * s) * (DM * 2), w < 7 ? 32 * DM * 2 : 0);
  const rsrc_t r2 = mk(w2 + (size_t)(ffn ? fi : 0) * 16 * (DM * 2), (ffn && w < 7) ? 16 * DM * 2 : 0);
  const int vo = (lane >> 3) * 4 * (DM * 2) + w * 128 + (lane & 7) * 16, v2 = (lane >> 3) * 2 * (DM * 2) + w * 128 + (lane & 7) * 16;
  u32x4 WO[4], W2[2];
  auto ldw = [&](int j) __attribute__((always_inline)) {
    if (j < 2) W2[j] = __builtin_amdgcn_raw_buffer_load_b128(r2, v2 + j * DM * 2, 0, NT);
    else WO[j - 2] = __builtin_amdgcn_raw_buffer_load_b128(ro, vo + (j - 2) * DM * 2, 0, NT);
  };

  auto build_a = [&](int j) __attribute__((always_inline)) {
    if ((j & 3) == 0) pp = *(const u32x4*)&S.p[w][2 * j];
    const u32 pr = pp[j & 3];
    A = u32x4{__builtin_amdgcn_perm(pr, pr, sel[0]), __builtin_amdgcn_perm(pr, pr, sel[1]), __builtin_amdgcn_perm(pr, pr, sel[2]),
              __builtin_amdgcn_perm(pr, pr, sel[3])};
  };
  auto pv = [&](int j) __attribute__((always_inline)) {
    acc = __builtin_amdgcn_mfma_f32_16x16x32_bf16(__builtin_bit_cast(bf16x8, A), __builtin_bit_cast(bf16x8, V[j]), acc, 0, 0, 0);
    if (j < 31) build_a(j + 1);
  };

  steps<3, 64>([&](auto I) __attribute__((always_inline)) {
    constexpr int i = I.v;
    if constexpr (i >= WIN0) pace<younger(i, i <= CUSE + 1 ? WIN0 : WIN)>();
    if constexpr (i > PUB) steer<i>(t0);
    SB();
    if constexpr (i > 3 && i < 32) K[i] = __builtin_amdgcn_raw_buffer_load_b128(rk, vv, kvo ^ mapi(i), NT);
    if constexpr (i >= 32) V[i - 32] = __builtin_amdgcn_raw_buffer_load_b128(rv, vv, kvo ^ mapi(i & 31), NT);
    SB();

    if constexpr (i == 63) {
      put(rws, (w == 6 && lane < 14) ? WS_T + (b * 56 + lane) * 1024 + (h * 8 + s) * 8 : NOWHERE, tag, now());
      SB();
    }
    if constexpr (i >= WAT && i < WAT + WN) { ldw(i - WAT); SB(); }
    if constexpr (i == 3) {
      if (w < 7) {
        float y[8], z[8];
        asm volatile("" : "+v"(z0), "+v"(z1));
        const float zf0 = asf(z0 << 16), zf1 = asf(z1 << 16);
#pragma unroll
        for (int j = 0; j < 8; ++j) {
          const u32 pq = __builtin_amdgcn_perm(a1[j >> 1], a0[j >> 1], (j & 1) ? 0x07060302u : 0x05040100u);
          y[j] = tr32(dot2(pq, x0, 0.f), dot2(pq, x1, 0.f));
          const float c = (j & 1) ? asf(c0[j >> 1] & 0xffff0000u) : asf(c0[j >> 1] << 16);
          z[j] = tr32(c * zf0, c * zf1);
        }
#pragma unroll
        for (int j = 0; j < 4; ++j) { y[j] = tr16(y[j], y[j + 4]); z[j] = tr16(z[j], z[j + 4]); }
#pragma unroll
        for (int j = 0; j < 2; ++j) { y[j] = trd<ROR8>(y[j], y[j + 2], lane & 8); z[j] = trd<ROR8>(z[j], z[j + 2], lane & 8); }
        const int e = (lane >> 5) * 64 + (lane & 7) * 8 + ((lane >> 4) & 1) * 4 + ((lane >> 3) & 1) * 2;
        *(f32x2*)&S.part[w][e] = f32x2{y[0], y[1]};
        *(f32x2*)&S.part[w][128 + e] = f32x2{z[0], z[1]};
      } else {
        float d = 0.f;
#pragma unroll
        for (int j = 0; j < 4; ++j) d = dot2(a0[j], a1[j], d);
        d = sum64(d);
        const u32 tg = (x0 * 0x9E3779B1u + 0x7F4A7C15u) | 1u;
        if (lane == 0) { S.gate = 1.f / (1.f + __expf(-d)); S.tag = tg; S.t0 = tstart; }
        if (__builtin_expect(__any(c0[0] == tg || c0[1] == tg || c0[2] == tg || c0[3] == tg), 0)) {
#pragma unroll
          for (int k = 0; k < 4; ++k) put(rws, oo + k * 512, 0u, 0u);
          asm volatile("s_waitcnt vmcnt(0)" ::: "memory");
        }
        asm volatile("" :: "v"(x1));
      }
    }
    if constexpr (i == PUB) {
      asm volatile("" :: "v"(code));
      lds_barrier();
      tag = __builtin_amdgcn_readfirstlane(S.tag);
      float t = 0.f;
#pragma unroll
      for (int j = 0; j < 7; ++j) t += S.part[j][tid & 255];
      const u32 dq = WS_Q + (h * 2 + w) * 8192 + (idx16 >> 2) * 2048 + (idx16 & 3) * 512 + lane * 8;
      const u32 dh = WS_H + ((w & 1) * 112 + (f2 >> 3) * 4 + (lane >> 4)) * 1024 + (f2 & 7) * 128 + (lane & 15) * 8;
      put(rws, w < 2 ? dq : (ffn && w < 4) ? dh : NOWHERE, tag, asu(t));
      put(rws, (w == 6 && (bid & 24) == 0 && lane < 16) ? WS_C + lane * 512 + ((bid >> 5) * 8 + (bid & 7)) * 8 : NOWHERE, tag, now());
    }
    if constexpr (i == CGET) gc = get(rws, w == 6 ? WS_C + idx16 * 512 + lane * 8 : NOWHERE);
    if constexpr (i == CUSE) {
      if (w == 6) {
        const bool ok = gc[1] == tag;
        const float n = sum64(ok ? 1.f : 0.f), d = sum64(ok ? (float)(int)(gc[0] - tstart) : 0.f);
        if (lane == 0 && n >= 16.f) __hip_atomic_store(&S.t0, tstart + (int)(d * __builtin_amdgcn_rcpf(n)) + CLEAD - due(PUB), RLX_GROUP);
      }
    }
    if constexpr (i == CUSE || i == CUSE + 1 || i == CUSE + 3) t0 = __builtin_amdgcn_readfirstlane(__hip_atomic_load(&S.t0, RLX_GROUP));
    if constexpr (i == QGET) {
      const u32 oh = (w == 4 && ffn) ? ohg : NOWHERE;
      gq[0] = get(rws, oqg); gq[1] = get(rws, oqg + 4096);
      gh[0] = get(rws, oh); gh[1] = get(rws, oh + 512);
    }
    if constexpr (i == QUSE) {
      settle<2, 8>(rws, oqg, 4096, tag, gq);
      float t = asf(gq[0][0]) + asf(gq[1][0]);
      t = tr32(t, t);
      if (lane < 32) S.q[w * 32 + lane] = (unsigned short)pk_bf16(t, 0.f);
      if (w == 4) {
        if (ffn) settle<2, 8>(rws, ohg, 512, tag, gh);
        float u = asf(gh[0][0]) + asf(gh[1][0]);
        u = tr16(u, u); u = tr32(u, u);
        if (lane < 16) S.hs[lane] = (u < 0.f ? 0.f : u) * (1.f - S.gate);
      }
    }
    if constexpr (i == QB) {
      lds_barrier();
      q = *(const u32x4*)&S.q[8 * (lane & 31)];
    }
    steps<0, 8>([&](auto T) __attribute__((always_inline)) {
      constexpr int t = T.v;
      if constexpr (i == score_step(t)) {
        float d[4];
#pragma unroll
        for (int u = 0; u < 4; ++u) {
          d[u] = 0.f;
#pragma unroll
          for (int r = 0; r < 4; ++r) d[u] = dot2(K[4 * t + u][r], q[r], d[u]);
        }
        sc[t] = trd<ROR8>(tr16(d[0], d[1]), tr16(d[2], d[3]), lane & 8);
        if constexpr (t & 1) sc[t] = trd<XOR7>(sc[t - 1], sc[t], lane & 4);
        if constexpr ((t & 3) == 3) sc[t] = trd<XOR2>(sc[t - 2], sc[t], lane & 2);
        asm volatile("" : "+v"(sc[t]));
      }
    });
    if constexpr (i == SM) {
      s2 = trd<XOR1>(sc[3], sc[7], lane & 1) * (0.0625f * 1.4426950408889634f);
      m = max64(s2);
      asm volatile("" : "+v"(s2), "+v"(m));
    }
    if constexpr (i == SM + 1) {
      const u32 pb = pk_bf16(__builtin_amdgcn_exp2f(s2 - m), 0.f);
      S.p[w][__builtin_bitreverse32(lane) >> 26] = (unsigned short)pb;
      const float l = sum64(asf(pb << 16));
      if (lane == 0) { S.mw[w] = m; S.lw[w] = l; }
      const int ii = lane & 15, g = lane >> 4, kk = g >> 1;
      const u32 two = (u32)((2 * kk + 1) << 8 | (2 * kk));
      const u32 act = (ii & 1) ? (two << 16 | 0x0c0cu) : (0x0c0c0000u | two);
#pragma unroll
      for (int r = 0; r < 4; ++r) sel[r] = ((ii >> 3) == (g & 1) && r == ((ii & 7) >> 1)) ? act : 0x0c0c0c0cu;
    }
    if constexpr (i == SM + 2) {
      asm volatile("s_waitcnt lgkmcnt(0)" ::: "memory");
      build_a(0);
      pv(0);
    }
    if constexpr (i >= SM + 2) pv(i - 32 - WIN);
    if constexpr (i == MB) {
      lds_barrier();
      const float mv = S.mw[lane & 7], M = max8(mv), L = sum8(S.lw[lane & 7] * __builtin_amdgcn_exp2f(mv - M));
      put(rws, (w == 7 && lane < 2) ? WS_ML + bh * 128 + s * 16 + lane * 8 : NOWHERE, tag, asu(lane ? L : M));
      fw = __builtin_amdgcn_exp2f(m - M);
      asm volatile("" : "+v"(fw));
    }
    SB();
  });

  const u32 oml = WS_ML + bh * 128 + (lane & 7) * 16;
  u32x2 gm[2];
  lf32x4* oa = (lf32x4*)&S.opart[w][128 * (lane >> 5) + 8 * (lane & 15) + 4 * ((lane >> 4) & 1)];
  asm volatile("" : "+v"(oa), "+v"(opub));
  steps<0, WIN>([&](auto E) __attribute__((always_inline)) {
    constexpr int e = E.v;
    pv(32 - WIN + e);
    SB();
    if constexpr (e == 0) {
      gm[0] = get(rws, w == 7 ? oml : NOWHERE); gm[1] = get(rws, w == 7 ? oml + 8 : NOWHERE);
    }
    if constexpr (e == WLATE) {
#pragma unroll
      for (int j = WN; j < 6; ++j) ldw(j);
    }
    SB();
  });
  *oa = acc * fw;
  lds_barrier();

  if (w < 7) {
    float v = 0.f;
    if (w < 4) {
      float o[8];
#pragma unroll
      for (int j = 0; j < 8; ++j) o[j] = S.opart[j][tid];
      v = ((o[0] + o[1]) + (o[2] + o[3])) + ((o[4] + o[5]) + (o[6] + o[7]));
    }
    put(rws, opub, tag, asu(v));
    asm volatile("" ::: "memory");
    SB();
    if (w == 6 && ffn) {
      u32x2 gt[2] = {get(rws, ot), get(rws, ot + 512)};
      settle<2, 8>(rws, ot, 512, tag, gt);
      const u32 t = now();
      const float age = -max64(-fminf((float)(t - gt[0][0]), (float)(t - gt[1][0])));
      if (lane == 0) S.fire = t + FLAG - (u32)fminf(age, (float)FLAG);
    }
    f32x2 r[4], wf[4][4];
    const f32x2 hv = *(const f32x2*)&S.hs[2 * (lane >> 3)];
#pragma unroll
    for (int j = 0; j < 4; ++j) {
      r[j] = hv[1] * widen(W2[1][j]) + hv[0] * widen(W2[0][j]);
#pragma unroll
      for (int i = 0; i < 4; ++i) wf[i][j] = widen(WO[i][j]);
      asm volatile("" : "+v"(r[j]), "+v"(wf[0][j]), "+v"(wf[1][j]), "+v"(wf[2][j]), "+v"(wf[3][j]));
    }
    u32 fo = fpub, tv = tag;
    lf32x4* os = (lf32x4*)&S.os[4 * (lane >> 3)];
    asm volatile("" : "+v"(fo), "+v"(os), "+v"(tv));
    lds_barrier();
    const f32x4 o = *os;
#pragma unroll
    for (int i = 0; i < 4; ++i)
#pragma unroll
      for (int j = 0; j < 4; ++j) r[j] = __builtin_elementwise_fma(f32x2{o[i], o[i]}, wf[i][j], r[j]);
    const float u0 = tr16(tr32(r[0][0], r[2][0]), tr32(r[1][0], r[3][0])), u1 = tr16(tr32(r[0][1], r[2][1]), tr32(r[1][1], r[3][1]));
    put(rws, fo, tv, asu(trd<ROR8>(u0, u1, lane & 8)));
    asm volatile(".Lcode_tail:");
  } else {
    u32 val[4];
    u64 got[4];
    u32x2 go[4];
    float wt[4];
    until(t0 + OGATE);
#pragma unroll
    for (int k = 0; k < 4; ++k) {
      got[k] = __builtin_amdgcn_ballot_w64(2 * k + (lane >> 5) == s);
      go[k] = get(rws, 2 * k + (lane >> 5) == s ? NOWHERE : oo + k * 512);
    }
    {
      const float* op = &S.opart[4 * (lane >> 5)][32 * s + (lane & 31)];
      float own = (op[0] + op[256]) + (op[512] + op[768]);
      own = tr32(own, own);
      settle<2, 8>(rws, oml, 8, tag, gm);
      const float ms = asf(gm[0][0]), M = max8(ms), e = __builtin_amdgcn_exp2f(ms - M), c = S.gate * e / sum8(asf(gm[1][0]) * e);
#pragma unroll
      for (int k = 0; k < 4; ++k) {
        wt[k] = asf(__builtin_amdgcn_ds_bpermute((2 * k + (lane >> 5)) * 4, asu(c)));
        val[k] = asu(own);
        asm volatile("" : "+v"(wt[k]));
      }
    }
    auto take = [&]() __attribute__((always_inline)) {
#pragma unroll
      for (int k = 0; k < 4; ++k) {
        const bool hit = go[k][1] == tag;
        val[k] = hit ? go[k][0] : val[k];
        got[k] |= __builtin_amdgcn_ballot_w64(hit);
      }
      return (got[0] & got[1] & got[2] & got[3]) == ~0ull;
    };
    lf32* os = (lf32*)&S.os[lane & 31];
    asm volatile("" : "+v"(os));
    SB();
    if (!take()) {
      u32 spins = 0;
      bool done;
#pragma clang loop unroll(disable)
      do {
        asm volatile("" ::: "memory");
#pragma unroll
        for (int k = 0; k < 4; ++k) go[k] = get(rws, __builtin_amdgcn_inverse_ballot_w64(got[k]) ? NOWHERE : oo + k * 512);
        done = take();
      } while (!done && ++spins < SPIN_CAP);
      if (!done) val[0] = QNAN;
    }
    float o = fmaf(asf(val[0]), wt[0], asf(val[1]) * wt[1]) + fmaf(asf(val[2]), wt[2], asf(val[3]) * wt[3]);
    o = tr32(o, o);
    *os = o;
    lds_barrier();
    if (ffn) {
      u32 ofg = WS_F + f2 * 4096 + lane * 8, oout = lane ? NOWHERE : f2 * 8;
      asm volatile("" : "+v"(ofg), "+v"(oout));
      const u32 fire = __builtin_amdgcn_readfirstlane(S.fire);
      until(t0 + FGATE);
      until(fire);
      u32x2 gf[8];
#pragma unroll
      for (int k = 0; k < 8; ++k) gf[k] = get(rws, ofg + k * 512);
      settle<8, 1>(rws, ofg, 512, tag, gf);
      float e = ((asf(gf[0][0]) + asf(gf[1][0])) + (asf(gf[2][0]) + asf(gf[3][0]))) + ((asf(gf[4][0]) + asf(gf[5][0])) + (asf(gf[6][0]) + asf(gf[7][0])));
      e = tr32(e, e); e = tr16(e, e);
      e += dpp<ROR8>(e); e += dpp<ROR4>(e);
      const u32 pk = pk_bf16(e, dpp<XOR1>(e));
      const u32 pk2 = asu(dpp<XOR2>(asf(pk)));
      __builtin_amdgcn_raw_buffer_store_b64(u32x2{pk, pk2}, mk(out, 2 * DM * 2), oout, 0, SC1);
      if (bid == 0 && lane == 0) __hip_atomic_store((gu32*)(ws + WS_EPOCH), tag, RLX_AGENT);
    }
  }
}

extern "C" void kernel_launch(void* const* d_in, const int* in_sizes, int n_in, void* d_out, int out_size, void* d_ws, size_t ws_size,
                              hipStream_t stream) {
  fused_decode<<<256, 512, 0, stream>>>((const char*)d_in[0], (const char*)d_in[1], (const char*)d_in[2], (const char*)d_in[3],
                                        (const char*)d_in[4], (const char*)d_in[6], (const char*)d_in[7], (const char*)d_in[8],
                                        (char*)d_out, (char*)d_ws);
}
```
